```python
import torch

def _softmax(x, axis=-1):
    return torch.softmax(x, dim=axis)

def _gelu(x):
    return torch.nn.functional.gelu(x, approximate='tanh')

def _silu(x):
    return torch.nn.functional.silu(x)

def _sigmoid(x):
    return torch.sigmoid(x)

def _layernorm(x, axis=-1, eps=1e-05):
    m = x.mean(dim=axis, keepdim=True)
    v = x.var(dim=axis, keepdim=True, unbiased=False)
    return (x - m) / torch.sqrt(v + eps)

def _scatter_add(tgt, idx, src, axis=0):
    return tgt.clone().index_add_(axis, idx, src)

def _segment_sum(src, seg, n):
    out = torch.zeros((n,) + src.shape[1:], dtype=src.dtype)
    return out.index_add_(0, seg, src)

def _pad(x, pw):
    flat = tuple((v for pair in reversed(pw) for v in pair))
    return torch.nn.functional.pad(x, flat)
kernel_name = 'adaLN_cond__ssm_conv_h3__moe_arch_mod__prompt'

def setup_inputs():

    _w = lambda a: (lambda t: t.to(torch.bfloat16) if t.is_floating_point() else t)(torch.as_tensor(a))

    def _A_setup():
        B, S, D = (1, 2048, 512)
        ks = 4
        return {'q': _w((lambda *s: torch.randn(*s, dtype=torch.bfloat16) * 0.1)(B, S, D)), 'k': _w((lambda *s: torch.randn(*s, dtype=torch.bfloat16) * 3.2)(B, S, D)), 'v': _w((lambda *s: torch.randn(*s, dtype=torch.bfloat16) * 3.2)(B, S, D)), 'shift_k': _w((lambda *s: torch.randn(*s, dtype=torch.bfloat16) * 3.2)(D, ks) * 0.5), 'ssm_k': _w((lambda *s: torch.randn(*s, dtype=torch.bfloat16) * 3.2)(D, S) * (1.0 / S ** 0.5)), 'ssm_D': _w((lambda *s: torch.randn(*s, dtype=torch.bfloat16) * 3.2)(D))}
    _A_in = _A_setup()

    def _B_setup():
        B, S, D, F, C = (1, 2048, 512, 1792, 8)
        return {'x': _w((lambda *s: torch.randn(*s, dtype=torch.bfloat16) * 0.1)(B, S, D)), 'w_r': _w((lambda *s: torch.randn(*s, dtype=torch.bfloat16) * 0.4)(D)), 'w_gate': _w((lambda *s: torch.randn(*s, dtype=torch.bfloat16) * 0.4)(D, F)), 'w_up': _w((lambda *s: torch.randn(*s, dtype=torch.bfloat16) * 0.4)(D, F)), 'w_down': _w((lambda *s: torch.randn(*s, dtype=torch.bfloat16) * 0.4)(F, D))}
    _B_in = _B_setup()
    return {'A_q': _A_in['q'], 'A_k': _A_in['k'], 'A_v': _A_in['v'], 'A_shift_k': _A_in['shift_k'], 'A_ssm_k': _A_in['ssm_k'], 'A_ssm_D': _A_in['ssm_D'], 'B_x': _B_in['x'], 'B_w_r': _B_in['w_r'], 'B_w_gate': _B_in['w_gate'], 'B_w_up': _B_in['w_up'], 'B_w_down': _B_in['w_down'], 'c': _w((lambda *s: torch.randn(*s, dtype=torch.bfloat16) * 0.2)(1, 512) * 8.0), 'w_mod': _w((lambda *s: torch.randn(*s, dtype=torch.bfloat16) * 0.2)(512, 6 * 512) * 512 ** (-0.5) * 8.0)}

def reference(A_q, A_k, A_v, A_shift_k, A_ssm_k, A_ssm_D, B_x, B_w_r, B_w_gate, B_w_up, B_w_down, c, w_mod):

    def _A_ref(q, k, v, shift_k, ssm_k, ssm_D):
        B, S, D = q.shape
        ks = shift_k.shape[-1]
        k_pad = _pad(k, ((0, 0), (ks - 1, 0), (0, 0)))
        k_shift = torch.zeros_like(k)
        for t in range(ks):
            k_shift = k_shift + k_pad[:, t:t + S, :] * shift_k[:, ks - 1 - t][None, None, :]
        kv = k_shift * v
        L = 2 * S
        kv_pad = _pad(kv, ((0, 0), (0, S), (0, 0)))
        kf_pad = _pad(ssm_k, ((0, 0), (0, S)))
        kv_f = (lambda x, axes=(-2, -1): torch.fft.rfft2(x.float(), dim=axes))(kv_pad[:, None, :, :], axes=(1, 2))
        kf_f = (lambda x, axes=(-2, -1): torch.fft.rfft2(x.float(), dim=axes))(kf_pad[:, None, :], axes=(1, 2))
        kf_f = (lambda x, *d: x.permute(*d))(kf_f, 1, 2, 0)
        y_f = kv_f * kf_f[None, :, :, :]
        y = (lambda x, s, axes=(-2, -1): torch.fft.irfft2(x, s=s, dim=axes).to(torch.bfloat16))(y_f, (1, L), axes=(1, 2))[:, 0, :S, :]
        y = y + kv * ssm_D[None, None, :]
        return q * y

    def _B_ref(x, w_r, w_gate, w_up, w_down):
        B, S, D = x.shape
        C = 8
        r = x @ w_r
        idx = torch.argsort(-r, axis=-1)[:, :C]
        idx_e = (lambda x, n, axis: torch.repeat_interleave(x, n, dim=axis))(idx.reshape(B, C, 1), D, axis=2)
        x_sel = (lambda x, i, axis: torch.take_along_dim(x, i, dim=axis))(x, idx_e, axis=1)
        g = _silu(x_sel @ w_gate)
        u = x_sel @ w_up
        y = g * u @ w_down
        r_sel = _sigmoid((lambda x, i, axis: torch.take_along_dim(x, i, dim=axis))(r, idx, axis=1))
        y = y * r_sel.reshape(B, C, 1)
        b_off = (torch.arange(B) * S).reshape(B, 1)
        flat_idx = (idx + b_off).reshape(B * C)
        zero = (x * 0.0).reshape(B * S, D)
        upd = _scatter_add(zero, flat_idx, y.reshape(B * C, D), axis=0)
        return x + upd.reshape(B, S, D)
    x = A_q
    mod = (_silu(c) @ w_mod).reshape(c.shape[0], 6, 1, -1)
    sa, ca, ga, sb, cb, gb = (mod[:, 0].clone(), mod[:, 1].clone(), mod[:, 2].clone(), mod[:, 3].clone(), mod[:, 4].clone(), mod[:, 5].clone())
    h = _layernorm(x, axis=-1) * (1 + ca) + sa
    x = x + ga * _A_ref(h, A_k, A_v, A_shift_k, A_ssm_k, A_ssm_D)
    h = _layernorm(x, axis=-1) * (1 + cb) + sb
    x = x + gb * _B_ref(h, B_w_r, B_w_gate, B_w_up, B_w_down)
    return x
```

```cpp
#include <hip/hip_runtime.h>
#include <stdint.h>

typedef unsigned short u16;
typedef short bf16x8 __attribute__((ext_vector_type(8)));
typedef float f32x4 __attribute__((ext_vector_type(4)));
typedef unsigned u32x4 __attribute__((ext_vector_type(4)));
typedef unsigned u32x2 __attribute__((ext_vector_type(2)));

constexpr int S = 2048, D = 512, F = 1792, TOPK = 8;

__device__ __forceinline__ float bf2f(unsigned h) { return __uint_as_float(h << 16); }
__device__ __forceinline__ float blo(unsigned w) { return __uint_as_float(w << 16); }
__device__ __forceinline__ float bhi(unsigned w) { return __uint_as_float(w & 0xffff0000u); }
__device__ __forceinline__ u16 f2bf(float f) { __bf16 b = (__bf16)f; return __builtin_bit_cast(u16, b); }
__device__ __forceinline__ float rb(float f) { return (float)(__bf16)f; }
__device__ __forceinline__ unsigned pk2(float lo, float hi) {
    typedef __bf16 bf2 __attribute__((ext_vector_type(2)));
    bf2 v; v[0] = (__bf16)lo; v[1] = (__bf16)hi;
    return __builtin_bit_cast(unsigned, v);
}
__device__ __forceinline__ float wsum(float v) {
#pragma unroll
    for (int o = 32; o >= 1; o >>= 1) v += __shfl_xor(v, o);
    return v;
}

constexpr size_t WS_MOD = 0;
constexpr size_t WS_U = 16384;
constexpr size_t WS_Y = WS_U + (size_t)D * S * 2;
constexpr size_t WS_H2 = WS_Y + (size_t)D * S * 2;
constexpr size_t WS_X1 = WS_H2 + (size_t)D * S * 2;
constexpr size_t WS_R = WS_X1 + (size_t)D * S * 2;
constexpr size_t WS_ACT = WS_R + 8192;
constexpr size_t WS_IDX = WS_ACT + 32768;

__global__ void __launch_bounds__(256) k_prep(const u16* __restrict__ k, const u16* __restrict__ v,
                                              const u16* __restrict__ shk, const u16* __restrict__ c,
                                              const u16* __restrict__ wmod, char* __restrict__ ws) {
    const int b = blockIdx.x, t = threadIdx.x;
    if (b < 256) {
        const int G = b & 7, j = b >> 3;
        const int cp = t & 31, rg = t >> 5;
        const int ch = 64 * G + 2 * cp;
        const int s0 = 64 * j + 8 * rg;
        unsigned kk[11], vv[8];
#pragma unroll
        for (int i = 0; i < 11; ++i) {
            int s = s0 - 3 + i;
            kk[i] = s >= 0 ? *(const unsigned*)(k + (size_t)s * D + ch) : 0u;
        }
#pragma unroll
        for (int i = 0; i < 8; ++i) vv[i] = *(const unsigned*)(v + (size_t)(s0 + i) * D + ch);
        u32x4 w = *(const u32x4*)(shk + ch * 4);
        float w0[4] = {blo(w[0]), bhi(w[0]), blo(w[1]), bhi(w[1])};
        float w1[4] = {blo(w[2]), bhi(w[2]), blo(w[3]), bhi(w[3])};
        float o0[8], o1[8];
#pragma unroll
        for (int i = 0; i < 8; ++i) {
            float a = rb(blo(kk[i]) * w0[3]);
            a = rb(a + rb(blo(kk[i + 1]) * w0[2]));
            a = rb(a + rb(blo(kk[i + 2]) * w0[1]));
            a = rb(a + rb(blo(kk[i + 3]) * w0[0]));
            o0[i] = a * blo(vv[i]);
            float bb = rb(bhi(kk[i]) * w1[3]);
            bb = rb(bb + rb(bhi(kk[i + 1]) * w1[2]));
            bb = rb(bb + rb(bhi(kk[i + 2]) * w1[1]));
            bb = rb(bb + rb(bhi(kk[i + 3]) * w1[0]));
            o1[i] = bb * bhi(vv[i]);
        }
        u16* U = (u16*)(ws + WS_U);
        u32x4 r0 = {pk2(o0[7], o0[6]), pk2(o0[5], o0[4]), pk2(o0[3], o0[2]), pk2(o0[1], o0[0])};
        u32x4 r1 = {pk2(o1[7], o1[6]), pk2(o1[5], o1[4]), pk2(o1[3], o1[2]), pk2(o1[1], o1[0])};
        *(u32x4*)(U + (size_t)ch * S + s0) = r0;
        *(u32x4*)(U + (size_t)(ch + 1) * S + s0) = r1;
    } else {
        __shared__ float red[32][65];
        const int cb = (b - 256) * 64;
        const int co = t & 7, kr = t >> 3;
        float acc[8] = {0, 0, 0, 0, 0, 0, 0, 0};
#pragma unroll
        for (int i = 0; i < 16; ++i) {
            int kk = kr + 32 * i;
            float cv = bf2f(c[kk]);
            float sc = rb(cv / (1.f + __expf(-cv)));
            u32x4 w = *(const u32x4*)(wmod + (size_t)kk * 3072 + cb + co * 8);
#pragma unroll
            for (int e = 0; e < 4; ++e) { acc[2 * e] += sc * blo(w[e]); acc[2 * e + 1] += sc * bhi(w[e]); }
        }
#pragma unroll
        for (int e = 0; e < 8; ++e) red[kr][co * 8 + e] = acc[e];
        __syncthreads();
        if (t < 64) {
            float s = 0;
            for (int i = 0; i < 32; ++i) s += red[i][t];
            ((float*)(ws + WS_MOD))[cb + t] = rb(s);
        }
    }
}

constexpr int U_PAD = 256;
constexpr int U_STRIDE = (U_PAD + S) * 2;
constexpr int KC_STRIDE = 4160;
__global__ void __launch_bounds__(256) k_conv(const u16* __restrict__ ssmk, const u16* __restrict__ ssmD, char* __restrict__ ws) {
    __shared__ __attribute__((aligned(16))) char lds[2 * U_STRIDE + 4 * KC_STRIDE + 16384];
    char* ldsU = lds;
    char* ldsK = lds + 2 * U_STRIDE;
    char* ldsX = ldsK + 4 * KC_STRIDE;
    const int b = blockIdx.x, t = threadIdx.x;
    const int ch0 = 64 * (b & 7) + 2 * (b >> 3);
    const int lane = t & 63, wave = t >> 6, chl = wave >> 1, h = wave & 1;
    const u16* U = (const u16*)(ws + WS_U);
#pragma unroll
    for (int i = 0; i < 2; ++i) {
        u32x4 x = *(const u32x4*)(U + (size_t)(ch0 + i) * S + t * 8);
        *(u32x4*)(ldsU + i * U_STRIDE + U_PAD * 2 + t * 16) = x;
    }
    if (t < 64) *(u32x4*)(ldsU + (t >> 5) * U_STRIDE + (t & 31) * 16) = (u32x4){0, 0, 0, 0};
#pragma unroll
    for (int i = 0; i < 2; ++i) {
        const u16* Kr = ssmk + (size_t)(ch0 + i) * S;
        u32x4 x = *(const u32x4*)(Kr + t * 8);
        unsigned nx = (t < 255) ? *(const unsigned*)(Kr + t * 8 + 8) : 0u;
        u32x4 y = {__builtin_amdgcn_alignbit(x[1], x[0], 16), __builtin_amdgcn_alignbit(x[2], x[1], 16),
                   __builtin_amdgcn_alignbit(x[3], x[2], 16), __builtin_amdgcn_alignbit(nx, x[3], 16)};
        *(u32x4*)(ldsK + (2 * i) * KC_STRIDE + 32 + t * 16) = x;
        *(u32x4*)(ldsK + (2 * i + 1) * KC_STRIDE + 32 + t * 16) = y;
        if (t == 0) {
            *(u32x4*)(ldsK + (2 * i) * KC_STRIDE) = (u32x4){0, 0, 0, 0};
            *(u32x4*)(ldsK + (2 * i) * KC_STRIDE + 16) = (u32x4){0, 0, 0, 0};
            *(u32x4*)(ldsK + (2 * i + 1) * KC_STRIDE) = (u32x4){0, 0, 0, 0};
            *(u32x4*)(ldsK + (2 * i + 1) * KC_STRIDE + 16) = (u32x4){0, 0, 0, x[0] << 16};
        }
    }
    __syncthreads();
    const int a = lane & 15, g = lane >> 4;
    u32x4 A[4][8];
    {
        const int x0 = 32 + a - 8 * g - 7;
        const int cpy = x0 & 1;
        const char* base = ldsK + (2 * chl + cpy) * KC_STRIDE + (x0 - cpy) * 2 + h * 8 * 32;
#pragma unroll
        for (int r = 0; r < 4; ++r)
#pragma unroll
            for (int mu = 0; mu < 8; ++mu) {
                const unsigned* p = (const unsigned*)(base + (16 * mu + 2 * r) * 32);
                A[r][mu] = (u32x4){p[0], p[1], p[2], p[3]};
            }
    }
    f32x4 acc[8];
#pragma unroll
    for (int i = 0; i < 8; ++i) acc[i] = (f32x4){0, 0, 0, 0};
    {
        const char* base = ldsU + chl * U_STRIDE + (U_PAD + 16 * (a - 1) + 8 * g - h * 8 * 16) * 2;
#pragma unroll
        for (int r = 0; r < 4; ++r) {
            u32x4 B[8];
#pragma unroll
            for (int tau = 0; tau < 8; ++tau) B[tau] = *(const u32x4*)(base + (256 * tau - 32 * r) * 2);
#pragma unroll
            for (int tau = 0; tau < 8; ++tau)
#pragma unroll
                for (int mu = 0; mu + tau < 8; ++mu)
                    acc[mu + tau] = __builtin_amdgcn_mfma_f32_16x16x32_bf16(__builtin_bit_cast(bf16x8, A[r][mu]), __builtin_bit_cast(bf16x8, B[tau]), acc[mu + tau], 0, 0, 0);
        }
    }
    f32x4 keep[4];
    {
        f32x4* X = (f32x4*)ldsX + wave * 256 + lane;
#pragma unroll
        for (int i = 0; i < 4; ++i) {
            f32x4 snd;
#pragma unroll
            for (int e = 0; e < 4; ++e) {
                float lo = acc[i][e], hi = acc[4 + i][e];
                asm volatile("" : "+v"(lo), "+v"(hi));
                snd[e] = h ? lo : hi;
                keep[i][e] = h ? hi : lo;
            }
            X[i * 64] = snd;
        }
    }
    __syncthreads();
    const float Dv = bf2f(ssmD[ch0 + chl]);
    u16* Y = (u16*)(ws + WS_Y);
    {
        const f32x4* X = (const f32x4*)ldsX + (wave ^ 1) * 256 + lane;
#pragma unroll
        for (int i = 0; i < 4; ++i) {
            f32x4 o = X[i * 64];
            o += keep[i];
            const int tt = 4 * h + i;
            const int s = 256 * tt + 16 * a + 4 * g;
            u32x2 kv = *(const u32x2*)(ldsU + chl * U_STRIDE + (U_PAD + (s & ~7) + 4 - 4 * (g & 1)) * 2);
            float y0 = rb(o[0]) + rb(bhi(kv[1]) * Dv);
            float y1 = rb(o[1]) + rb(blo(kv[1]) * Dv);
            float y2 = rb(o[2]) + rb(bhi(kv[0]) * Dv);
            float y3 = rb(o[3]) + rb(blo(kv[0]) * Dv);
            u32x2 out = {pk2(y0, y1), pk2(y2, y3)};
            *(u32x2*)(Y + (size_t)(s >> 3) * 4096 + (ch0 + chl) * 8 + (s & 7)) = out;
        }
    }
}

__device__ __forceinline__ void ln_stats(const float* x, float& m, float& sd) {
    float s = 0;
#pragma unroll
    for (int i = 0; i < 8; ++i) s += x[i];
    s = wsum(s);
    float mean = s * (1.f / 512.f);
    float q = 0;
#pragma unroll
    for (int i = 0; i < 8; ++i) { float d = x[i] - mean; q += d * d; }
    q = wsum(q);
    float var = rb(q * (1.f / 512.f));
    m = rb(mean);
    sd = rb(sqrtf(rb(var + 1e-5f)));
}

__global__ void __launch_bounds__(512) k_row(const u16* __restrict__ xq, const u16* __restrict__ wr, char* __restrict__ ws, u16* __restrict__ out) {
    __shared__ __attribute__((aligned(16))) u16 ly[512 * 8];
    const int b = blockIdx.x, t = threadIdx.x, lane = t & 63, wave = t >> 6;
    const u16* Y = (const u16*)(ws + WS_Y);
    *(u32x4*)(ly + t * 8) = *(const u32x4*)(Y + (size_t)b * 4096 + t * 8);
    const int row = b * 8 + wave;
    const int c0 = lane * 8;
    u32x4 xw = *(const u32x4*)(xq + (size_t)row * D + c0);
    float x[8];
#pragma unroll
    for (int e = 0; e < 4; ++e) { x[2 * e] = blo(xw[e]); x[2 * e + 1] = bhi(xw[e]); }
    const float* mod = (const float*)(ws + WS_MOD);
    float m, sd;
    ln_stats(x, m, sd);
    __syncthreads();
    float x1[8];
#pragma unroll
    for (int e = 0; e < 8; ++e) {
        float sa = mod[c0 + e], ca = mod[512 + c0 + e], ga = mod[1024 + c0 + e];
        float hh = rb(rb(rb(rb(x[e] - m) / sd) * rb(1.f + ca)) + sa);
        float y = bf2f(ly[(c0 + e) * 8 + wave]);
        x1[e] = rb(x[e] + rb(ga * rb(hh * y)));
    }
    ln_stats(x1, m, sd);
    float h2[8], o[8];
    float r = 0;
    u32x4 ww = *(const u32x4*)(wr + c0);
#pragma unroll
    for (int e = 0; e < 8; ++e) {
        float sb = mod[1536 + c0 + e], cb = mod[2048 + c0 + e], gb = mod[2560 + c0 + e];
        h2[e] = rb(rb(rb(rb(x1[e] - m) / sd) * rb(1.f + cb)) + sb);
        float w = (e & 1) ? bhi(ww[e >> 1]) : blo(ww[e >> 1]);
        r += h2[e] * w;
        o[e] = x1[e] + rb(gb * h2[e]);
    }
    r = wsum(r);
    if (lane == 0) ((float*)(ws + WS_R))[row] = rb(r);
    *(u32x4*)((u16*)(ws + WS_H2) + (size_t)row * D + c0) = (u32x4){pk2(h2[0], h2[1]), pk2(h2[2], h2[3]), pk2(h2[4], h2[5]), pk2(h2[6], h2[7])};
    *(u32x4*)((u16*)(ws + WS_X1) + (size_t)row * D + c0) = (u32x4){pk2(x1[0], x1[1]), pk2(x1[2], x1[3]), pk2(x1[4], x1[5]), pk2(x1[6], x1[7])};
    *(u32x4*)(out + (size_t)row * D + c0) = (u32x4){pk2(o[0], o[1]), pk2(o[2], o[3]), pk2(o[4], o[5]), pk2(o[6], o[7])};
}

__device__ __forceinline__ void topk_wave(const float* R, int lane, int* sidx, float* sval) {
    unsigned key[32];
#pragma unroll
    for (int i = 0; i < 8; ++i) {
        f32x4 v = *(const f32x4*)(R + i * 256 + lane * 4);
#pragma unroll
        for (int e = 0; e < 4; ++e) {
            unsigned u = __float_as_uint(v[e]);
            u = (u & 0x80000000u) ? ~u : (u | 0x80000000u);
            key[i * 4 + e] = (u & 0xffff0000u) | (unsigned)(2047 - (i * 256 + lane * 4 + e));
        }
    }
    for (int it = 0; it < TOPK; ++it) {
        unsigned m = 0;
#pragma unroll
        for (int i = 0; i < 32; ++i) m = max(m, key[i]);
#pragma unroll
        for (int o = 32; o >= 1; o >>= 1) m = max(m, (unsigned)__shfl_xor((int)m, o));
#pragma unroll
        for (int i = 0; i < 32; ++i) key[i] = key[i] == m ? 0u : key[i];
        if (lane == 0) {
            int idx = 2047 - (int)(m & 0xffffu);
            sidx[it] = idx;
            unsigned u = m & 0xffff0000u;
            u = (u & 0x80000000u) ? (u & 0x7fffffffu) : (~u & 0xffff0000u);
            sval[it] = __uint_as_float(u);
        }
    }
}

__global__ void __launch_bounds__(256) k_gateup(const u16* __restrict__ wg, const u16* __restrict__ wu, char* __restrict__ ws) {
    __shared__ int sidx[8];
    __shared__ float sval[8];
    __shared__ float xs[8][512];
    __shared__ float red[32][32][9];
    const int b = blockIdx.x, t = threadIdx.x;
    const int cp = t & 7, ks = t >> 3;
    const int f0 = b * 16 + cp * 2;
    unsigned g[16], u[16];
#pragma unroll
    for (int i = 0; i < 16; ++i) {
        g[i] = *(const unsigned*)(wg + (size_t)(ks * 16 + i) * F + f0);
        u[i] = *(const unsigned*)(wu + (size_t)(ks * 16 + i) * F + f0);
    }
    if (t < 64) topk_wave((const float*)(ws + WS_R), t, sidx, sval);
    __syncthreads();
    if (b == 0 && t < 8) { ((int*)(ws + WS_IDX))[t] = sidx[t]; ((float*)(ws + WS_IDX))[8 + t] = sval[t]; }
    const u16* H2 = (const u16*)(ws + WS_H2);
#pragma unroll
    for (int i = 0; i < 8; ++i) {
        unsigned w = *(const unsigned*)(H2 + (size_t)sidx[i] * D + t * 2);
        xs[i][t * 2] = blo(w); xs[i][t * 2 + 1] = bhi(w);
    }
    __syncthreads();
    float acc[4][8];
#pragma unroll
    for (int q = 0; q < 4; ++q)
#pragma unroll
        for (int r = 0; r < 8; ++r) acc[q][r] = 0;
#pragma unroll
    for (int i = 0; i < 16; ++i) {
#pragma unroll
        for (int r = 0; r < 8; ++r) {
            float xv = xs[r][ks * 16 + i];
            acc[0][r] += xv * blo(g[i]); acc[1][r] += xv * bhi(g[i]);
            acc[2][r] += xv * blo(u[i]); acc[3][r] += xv * bhi(u[i]);
        }
    }
#pragma unroll
    for (int q = 0; q < 4; ++q)
#pragma unroll
        for (int r = 0; r < 8; ++r) red[ks][cp * 4 + q][r] = acc[q][r];
    __syncthreads();
    if (t < 128) {
        const int col = t & 15, r = t >> 4;
        float sg = 0, su = 0;
        for (int i = 0; i < 32; ++i) { sg += red[i][(col >> 1) * 4 + (col & 1)][r]; su += red[i][(col >> 1) * 4 + 2 + (col & 1)][r]; }
        sg = rb(sg); su = rb(su);
        float gg = rb(sg / (1.f + __expf(-sg)));
        ((u16*)(ws + WS_ACT))[r * F + b * 16 + col] = f2bf(gg * su);
    }
}

__global__ void __launch_bounds__(256) k_down(const u16* __restrict__ wd, char* __restrict__ ws, u16* __restrict__ out) {
    __shared__ float red[64][65];
    const int b = blockIdx.x, t = threadIdx.x, lane = t & 63, wave = t >> 6;
    u32x4 w[7];
#pragma unroll
    for (int i = 0; i < 7; ++i) w[i] = *(const u32x4*)(wd + (size_t)(t + 256 * i) * D + b * 8);
    const u16* act = (const u16*)(ws + WS_ACT);
    float acc[8][8];
#pragma unroll
    for (int r = 0; r < 8; ++r)
#pragma unroll
        for (int e = 0; e < 8; ++e) acc[r][e] = 0;
#pragma unroll
    for (int i = 0; i < 7; ++i)
#pragma unroll
        for (int r = 0; r < 8; ++r) {
            float av = bf2f(act[r * F + t + 256 * i]);
#pragma unroll
            for (int e = 0; e < 4; ++e) { acc[r][2 * e] += av * blo(w[i][e]); acc[r][2 * e + 1] += av * bhi(w[i][e]); }
        }
    for (int ww = 0; ww < 4; ++ww) {
        if (wave == ww) {
#pragma unroll
            for (int r = 0; r < 8; ++r)
#pragma unroll
                for (int e = 0; e < 8; ++e) {
                    if (ww == 0) red[r * 8 + e][lane] = acc[r][e]; else red[r * 8 + e][lane] += acc[r][e];
                }
        }
        __syncthreads();
    }
    if (t < 64) {
        float s = 0;
        for (int i = 0; i < 64; ++i) s += red[t][i];
        const int r = t >> 3, col = b * 8 + (t & 7);
        const int idx = ((const int*)(ws + WS_IDX))[r];
        const float rv = ((const float*)(ws + WS_IDX))[8 + r];
        float sg = rb(1.f / (1.f + __expf(-rv)));
        float y = rb(rb(s) * sg);
        float h2 = bf2f(((const u16*)(ws + WS_H2))[(size_t)idx * D + col]);
        float x1 = bf2f(((const u16*)(ws + WS_X1))[(size_t)idx * D + col]);
        float gb = ((const float*)(ws + WS_MOD))[2560 + col];
        out[(size_t)idx * D + col] = f2bf(x1 + rb(gb * rb(h2 + y)));
    }
}

extern "C" void kernel_launch(void* const* d_in, const int* in_sizes, int n_in,
                              void* d_out, int out_size, void* d_ws, size_t ws_size,
                              hipStream_t stream) {
    const u16* q = (const u16*)d_in[0];
    const u16* k = (const u16*)d_in[1];
    const u16* v = (const u16*)d_in[2];
    const u16* shk = (const u16*)d_in[3];
    const u16* ssmk = (const u16*)d_in[4];
    const u16* ssmD = (const u16*)d_in[5];
    const u16* wr = (const u16*)d_in[7];
    const u16* wg = (const u16*)d_in[8];
    const u16* wu = (const u16*)d_in[9];
    const u16* wd = (const u16*)d_in[10];
    const u16* c = (const u16*)d_in[11];
    const u16* wmod = (const u16*)d_in[12];
    char* ws = (char*)d_ws;
    u16* out = (u16*)d_out;
    k_prep<<<256 + 48, 256, 0, stream>>>(k, v, shk, c, wmod, ws);
    k_conv<<<256, 256, 0, stream>>>(ssmk, ssmD, ws);
    k_row<<<256, 512, 0, stream>>>(q, wr, ws, out);
    k_gateup<<<112, 256, 0, stream>>>(wg, wu, ws);
    k_down<<<64, 256, 0, stream>>>(wd, ws, out);
}
```

```cpp
#include <hip/hip_runtime.h>
#include <stdint.h>
#pragma clang diagnostic ignored "-Wint-to-pointer-cast"

typedef unsigned short u16;
typedef unsigned long long u64;
typedef short bf16x8 __attribute__((ext_vector_type(8)));
typedef short s16x4 __attribute__((ext_vector_type(4)));
typedef __bf16 bf2 __attribute__((ext_vector_type(2)));
typedef float f32x4 __attribute__((ext_vector_type(4)));
typedef float f32x2 __attribute__((ext_vector_type(2)));
typedef unsigned u32x4 __attribute__((ext_vector_type(4)));
typedef unsigned u32x2 __attribute__((ext_vector_type(2)));
#define LAS __attribute__((address_space(3)))
#define GAS __attribute__((address_space(1)))

constexpr int S = 2048, D = 512, F = 1792;
constexpr unsigned SENT = 0xFFFFFFFFu;

constexpr unsigned WS_E = 0;
constexpr unsigned WS_C2 = 1024;
constexpr unsigned WS_F1 = 2048;
constexpr unsigned WS_F2 = WS_F1 + 256 * 128;
constexpr unsigned WS_F3 = WS_F2 + 64 * 128;
constexpr unsigned WS_MOD = WS_F3 + 128 * 128;
constexpr unsigned WS_R = WS_MOD + 12288;
constexpr unsigned WS_ACT = WS_R + 16384;
constexpr unsigned WS_MP = 131072;
constexpr unsigned WS_U = WS_MP + 393216;
constexpr unsigned WS_Y = WS_U + 2097152;
constexpr unsigned WS_H = WS_Y + 2097152;
constexpr unsigned WS_END = WS_H + 2097152;
constexpr unsigned WS_DBG = WS_END;
static_assert(WS_ACT + 28672 <= WS_MP, "ws layout");

constexpr int U_PADB = 512, U_STRIDE = 4672;
constexpr int L_U = 0;
constexpr int L_P = 9344;
constexpr int L_MISC = L_P + 4 * 9216 + 128;
constexpr int L_S = L_MISC + 2048;
constexpr int KROW = 192, SW = 36 * KROW;
constexpr int KC1 = 2368, KCW = 4736;
constexpr int L_KC = L_S + 32768;
constexpr int L_T = L_KC + 4 * KCW;
constexpr int L_CH = L_T + 8 * 2064;
constexpr int L_X = L_CH + 12288;
constexpr int L_W = L_S;
constexpr int L_END = L_X + 16384;
static_assert(L_W + 65536 <= L_CH, "lds layout");
static_assert(L_END <= 160 * 1024 && L_END > 80 * 1024, "lds size");
constexpr int M_WMAX = L_MISC;
constexpr int M_SEL = L_MISC + 64;
constexpr int M_CNT0 = L_MISC + 96;
constexpr int M_SUMS = L_MISC + 128;
constexpr int M_CAND = L_MISC + 768;
constexpr int M_MODP = L_MISC + 512;

#define APOLL 3
#define D2 185
#define D3 0
#define D4 0
constexpr unsigned SPIN_CAP = 40000;

__device__ __forceinline__ float blo(unsigned w) { return __uint_as_float(w << 16); }
__device__ __forceinline__ float bhi(unsigned w) { return __uint_as_float(w & 0xffff0000u); }
__device__ __forceinline__ f32x2 b2(unsigned w) { return (f32x2){blo(w), bhi(w)}; }
__device__ __forceinline__ float rb(float f) { return (float)(__bf16)f; }
__device__ __forceinline__ unsigned pk2(float lo, float hi) {
    bf2 v; v[0] = (__bf16)lo; v[1] = (__bf16)hi;
    return __builtin_bit_cast(unsigned, v);
}
__device__ __forceinline__ float dot2(unsigned a, unsigned b, float c) {
    return __builtin_amdgcn_fdot2_f32_bf16(__builtin_bit_cast(bf2, a), __builtin_bit_cast(bf2, b), c, false);
}
__device__ __forceinline__ f32x2 pfma(f32x2 a, f32x2 b, f32x2 c) { return __builtin_elementwise_fma(a, b, c); }
__device__ __forceinline__ unsigned rt32() { return (unsigned)__builtin_amdgcn_s_memrealtime(); }
#define BAR() do { asm volatile("s_waitcnt lgkmcnt(0)" ::: "memory"); __builtin_amdgcn_s_barrier(); asm volatile("" ::: "memory"); } while (0)
#define DRAIN() asm volatile("s_waitcnt vmcnt(0)" ::: "memory")
#define CFENCE() asm volatile("" ::: "memory")
#define PIN(x) asm volatile("" : "+v"(x))
#define PINS(x) asm volatile("" : "+s"(x))
#define KEEP(x) asm volatile("" :: "v"(x))
#define SCHED() __builtin_amdgcn_sched_barrier(0)
#define SPIN _Pragma("unroll 1") for (unsigned sp_ = 0; sp_ < SPIN_CAP; ++sp_)
#define LDS(T, off) (*(LAS T*)(unsigned)(off))
template <int OFF>
__device__ __forceinline__ unsigned lds_arrive() {
    unsigned old; u64 ex;
    asm volatile("s_mov_b64 %1, exec\n\ts_mov_b64 exec, 1\n\tds_add_rtn_u32 %0, %2, %3 offset:%4\n\ts_mov_b64 exec, %1\n\ts_waitcnt lgkmcnt(0)"
                 : "=&v"(old), "=&s"(ex) : "v"(0u), "v"(1u), "i"(OFF) : "memory");
    return __builtin_amdgcn_readfirstlane(old);
}
template <int OFF>
__device__ __forceinline__ void lds_wait(unsigned want) {
#pragma unroll 1
    for (unsigned sp_ = 0; sp_ < 4000000u; ++sp_) { CFENCE(); if (__builtin_amdgcn_readfirstlane(LDS(volatile unsigned, OFF)) >= want) break; }
    CFENCE();
}
__device__ __forceinline__ float smul(float a, float b) { float r; asm("v_mul_f32 %0, %1, %2" : "=v"(r) : "v"(a), "v"(b)); return r; }
__device__ __forceinline__ float sadd(float a, float b) { float r; asm("v_add_f32 %0, %1, %2" : "=v"(r) : "v"(a), "v"(b)); return r; }

template <int CTRL, int RMASK = 0xf>
__device__ __forceinline__ float dpp_add(float v) {
    return v + __int_as_float(__builtin_amdgcn_update_dpp(0, __float_as_int(v), CTRL, RMASK, 0xf, true));
}
__device__ __forceinline__ float half_sum(float v) {
    v = dpp_add<0x111>(v); v = dpp_add<0x112>(v); v = dpp_add<0x114>(v); v = dpp_add<0x118>(v);
    return dpp_add<0x142>(v);
}
__device__ __forceinline__ float fold2(float a, float b) {
    auto r = __builtin_amdgcn_permlane32_swap(__float_as_uint(a), __float_as_uint(b), false, false);
    return __uint_as_float(r[0]) + __uint_as_float(r[1]);
}
__device__ __forceinline__ float rdl(float v, int l) { return __int_as_float(__builtin_amdgcn_readlane(__float_as_int(v), l)); }
template <int CTRL, int RMASK = 0xf>
__device__ __forceinline__ unsigned dpp_max(unsigned v) {
    return max(v, (unsigned)__builtin_amdgcn_update_dpp(0, (int)v, CTRL, RMASK, 0xf, true));
}
__device__ __forceinline__ unsigned wave_max_v(unsigned v) {
    v = dpp_max<0x111>(v); v = dpp_max<0x112>(v); v = dpp_max<0x114>(v); v = dpp_max<0x118>(v);
    v = dpp_max<0x142>(v); v = dpp_max<0x143>(v);
    return v;
}
__device__ __forceinline__ unsigned wave_max(unsigned v) { return (unsigned)__builtin_amdgcn_readlane((int)wave_max_v(v), 63); }
__device__ __forceinline__ u32x2 trd(unsigned off) {
    return __builtin_bit_cast(u32x2, __builtin_amdgcn_ds_read_tr16_b64_v4i16((LAS s16x4*)off));
}
__device__ __forceinline__ u32x4 trd2(unsigned off, unsigned d) { const u32x2 lo = trd(off), hi = trd(off + d); return (u32x4){lo[0], lo[1], hi[0], hi[1]}; }
__device__ __forceinline__ f32x4 mfma(u32x4 a, u32x4 b, f32x4 c) {
    return __builtin_amdgcn_mfma_f32_16x16x32_bf16(__builtin_bit_cast(bf16x8, a), __builtin_bit_cast(bf16x8, b), c, 0, 0, 0);
}
__device__ __forceinline__ f32x4 mfma16(u32x2 a, u32x2 b, f32x4 c) {
    return __builtin_amdgcn_mfma_f32_16x16x16bf16_1k(__builtin_bit_cast(s16x4, a), __builtin_bit_cast(s16x4, b), c, 0, 0, 0);
}
__device__ __forceinline__ float silu(float x) { return x * __builtin_amdgcn_rcpf(1.f + __expf(-x)); }
__device__ __forceinline__ unsigned max3u(unsigned a, unsigned b, unsigned c) { return max(max(a, b), c); }
__device__ __forceinline__ unsigned max4(u32x4 v, unsigned m) { return max3u(max3u(v[0], v[1], m), v[2], v[3]); }
__device__ __forceinline__ bool none(bool c) { return __builtin_amdgcn_ballot_w64(c) == 0ull; }
__device__ __forceinline__ void wait_until(unsigned t) {
    const unsigned now = rt32();
    if ((int)(t - now) > 400) t = now + 400;
#pragma unroll 1
    while ((int)(rt32() - t) < 0) {}
}

#define STAMP(i) do {} while (0)
#define NST 0

#define LD16(rs, off) __builtin_amdgcn_raw_buffer_load_b128(rs, off, 0, 16)
#define LD8(rs, off) __builtin_amdgcn_raw_buffer_load_b64(rs, off, 0, 16)
#define LD4(rs, off) __builtin_amdgcn_raw_buffer_load_b32(rs, off, 0, 16)
#define ST16(v, rs, off) __builtin_amdgcn_raw_buffer_store_b128(v, rs, off, 0, 16)
#define ST8(v, rs, off) __builtin_amdgcn_raw_buffer_store_b64(v, rs, off, 0, 16)
#define ST4(v, rs, off) __builtin_amdgcn_raw_buffer_store_b32(v, rs, off, 0, 16)
#define SENT4 ((u32x4){SENT, SENT, SENT, SENT})
#define GLD(T, p, off) (*(const GAS T*)((const GAS char*)(p) + (off)))

__global__ void __launch_bounds__(512, 2)
fused_kernel(const u16* __restrict__ kin, const u16* __restrict__ vin, char* __restrict__ ws, const u16* __restrict__ ssmk,
             const u16* __restrict__ shk, const u16* __restrict__ cin, const u16* __restrict__ wmod, const u16* __restrict__ ssmD,
             const u16* __restrict__ xq, const u16* __restrict__ wr, const u16* __restrict__ wg, const u16* __restrict__ wu,
             const u16* __restrict__ wd, u16* __restrict__ out) {
    __shared__ __attribute__((aligned(16))) char lds_[L_END];
    asm volatile("" :: "v"((LAS char*)lds_) : "memory");
    const int b = blockIdx.x, t = threadIdx.x, lane = t & 63;
    const int wave = __builtin_amdgcn_readfirstlane(t >> 6);
    const int G = b & 7, j = b >> 3;
    const int ch0 = 64 * G + 2 * j;
    PINS(kin); PINS(vin); PINS(ws); PINS(ssmk); PINS(shk); PINS(cin); PINS(wmod); PINS(ssmD);
    PINS(xq); PINS(wr); PINS(wg); PINS(wu); PINS(wd); PINS(out);
    const auto rs = __builtin_amdgcn_make_buffer_rsrc(ws, 0, WS_END, 0x00020000);
    STAMP(0);
    const bool is_row = j < 16;
    const int f = j - 16;
    const unsigned yb = WS_Y + (G * 32 + j) * 8192;
    const int a = lane & 15, g = lane >> 4;
    unsigned ep, dd;
    unsigned tag;
    if (wave < 4) {
        const int hw = wave;
        {
            const unsigned toff = ((64 * j + 16 * hw + (lane >> 3)) * D + 64 * G + 8 * (lane & 7)) * 2;
            const u32x4 ka = GLD(u32x4, kin, toff), va = GLD(u32x4, vin, toff);
            const u32x4 kb = GLD(u32x4, kin, toff + 8192), vb = GLD(u32x4, vin, toff + 8192);
            u32x4 kh = {0, 0, 0, 0};
            if (lane < 32 && toff >= 4096u) kh = GLD(u32x4, kin, toff - 4096u);
            const u32x2 shw = GLD(u32x2, shk, (64 * G + lane) * 8);
            CFENCE();
            asm volatile("s_load_dword %0, %2, 0x0\n\ts_load_dword %1, %3, 0x0" : "=&s"(ep), "=&s"(dd) : "s"(ws), "s"(ssmD + ch0) : "memory");
            unsigned uo = WS_U + (16 * G + (lane >> 2)) * 16384 + j * 512 + hw * 128 + (lane & 3) * 16;
            const int q = a >> 2;
            unsigned sw = L_S + hw * SW + (lane >> 3) * KROW + (lane & 7) * 16;
            unsigned ra = L_S + hw * SW + q * KROW + 32 * g + 8 * (lane & 3);
            unsigned rb_ = ra + (17 - (q >> 1)) * KROW;
            f32x2 o[8];
#pragma unroll
            for (int i = 0; i < 8; ++i) { o[i] = (f32x2){0.f, 0.f}; PIN(o[i]); }
            PIN(sw); PIN(ra); PIN(rb_); PIN(uo);
            BAR();
            asm volatile("" : "+s"(ep), "+s"(dd));
            tag = ep + 1u;
            LDS(u32x4, sw + 4 * KROW) = ka; LDS(u32x4, sw + 20 * KROW) = va;
            LDS(u32x4, sw + 12 * KROW) = kb; LDS(u32x4, sw + 28 * KROW) = vb;
            if (lane < 32) LDS(u32x4, sw) = kh;
            CFENCE();
            STAMP(1);
            const unsigned w32 = __builtin_amdgcn_alignbit(shw[1], shw[1], 16), w10 = __builtin_amdgcn_alignbit(shw[0], shw[0], 16);
            unsigned Q[18], V[8];
#pragma unroll
            for (int i = 0; i < 4; ++i) {
                const u32x2 qo = trd(ra + (1 + 4 * i) * KROW), qe = trd(ra + (2 + 4 * i) * KROW), vv = trd(ra + (20 + 4 * i) * KROW);
                Q[4 * i] = qo[0]; Q[4 * i + 2] = qo[1]; Q[4 * i + 1] = qe[0]; Q[4 * i + 3] = qe[1]; V[2 * i] = vv[0]; V[2 * i + 1] = vv[1];
            }
            { const u32x2 ql = trd(rb_); Q[16] = ql[0]; Q[17] = ql[1]; }
            const unsigned cl = LDS(volatile unsigned, M_CNT0);
#pragma unroll
            for (int hh = 0; hh < 2; ++hh) {
#pragma unroll
                for (int i = 8 * hh; i < 8 * hh + 8; ++i) o[i >> 1][i & 1] = dot2(Q[i + 2], w10, dot2(Q[i], w32, o[i >> 1][i & 1]));
#pragma unroll
                for (int i = 4 * hh; i < 4 * hh + 4; ++i) o[i] *= b2(V[i]);
                u32x4 r = {pk2(o[4 * hh + 3][1], o[4 * hh + 3][0]), pk2(o[4 * hh + 2][1], o[4 * hh + 2][0]), pk2(o[4 * hh + 1][1], o[4 * hh + 1][0]), pk2(o[4 * hh][1], o[4 * hh][0])};
                if (hh == 0) { PIN(r); if (__builtin_amdgcn_readfirstlane(cl) < 4u) lds_wait<M_CNT0>(4u); }
                ST16(r, rs, uo + 64 * hh);
                SCHED();
            }
            CFENCE();
            STAMP(2);
        }
        unsigned ub = WS_U + (16 * G + (j >> 1)) * 16384 + (j & 1) * 32 + (hw * 64 + (lane >> 1)) * 64 + (lane & 1) * 16;
        unsigned ul = L_U + (lane & 1) * U_STRIDE + U_PADB + (hw * 64 + (lane >> 1)) * 16;
        const u16* pfa = hw == 0 ? wg : hw == 1 ? wu : wd;
        unsigned pfo = hw < 2 ? (16 * j + (lane >> 2)) * (F * 2) + ((448 * G) / 128 + (lane & 3)) * 128
                              : (56 * j + min(lane, 55)) * (D * 2) + 128 * G;
        unsigned bo = is_row ? ((G * 16 + j) * 16 + 4 * hw) * 1024 + lane * 16
                             : WS_MP + (G * 32 + 4 * (lane >> 3)) * 1536 + f * 128 + (lane & 7) * 16;
        const int chl = (t >> 1) & 1, half = t & 1;
        unsigned pr = L_P + chl * (2 * 9216 + 128) + hw * 1152 + half * 576 + (lane >> 2) * 16;
        unsigned yo = yb + t * 16;
        unsigned kvo = L_U + chl * U_STRIDE + U_PADB + (t >> 2) * 32 + half * 16;
        float Dv = chl ? bhi(dd) : blo(dd);
        PIN(ub); PIN(ul); PIN(pfo); PIN(bo); PIN(pr); PIN(yo); PIN(kvo); PIN(Dv);
        DRAIN();
        STAMP(3);
        lds_wait<M_CNT0 + 4>(4u);
        STAMP(4);
        u32x4 u0, u1;
        SPIN {
            CFENCE();
            u0 = LD16(rs, ub); u1 = LD16(rs, ub + 2048);
            if (none(max4(u0, max4(u1, 0u)) == SENT)) break;
        }
        LDS(u32x4, ul) = u0; LDS(u32x4, ul + 512) = u1;
        BAR();
        const unsigned tconv = rt32();
        STAMP(5);
        unsigned pf = 0;
        u32x4 xr[4];
        if (is_row) {
#pragma unroll
            for (int i = 0; i < 4; ++i) xr[i] = GLD(u32x4, xq, bo + i * 1024);
        } else if (hw == 3 && f < 12) {
#pragma unroll
            for (int i = 0; i < 4; ++i) xr[i] = LD16(rs, bo + i * 1536);
        }
        CFENCE();
        ST16(SENT4, rs, yo); ST16(SENT4, rs, yo + 4096);
        CFENCE();
        float kd[2][8];
#pragma unroll
        for (int i = 0; i < 2; ++i) {
            const u32x4 kv = LDS(u32x4, kvo + i * 2048);
#pragma unroll
            for (int e = 0; e < 4; ++e) { kd[i][2 * e] = smul(bhi(kv[3 - e]), Dv); kd[i][2 * e + 1] = smul(blo(kv[3 - e]), Dv); PIN(kd[i][2 * e]); PIN(kd[i][2 * e + 1]); }
        }
        DRAIN();
        if (lds_arrive<M_CNT0 + 8>() == 3u) {
            unsigned old = 0;
            if (lane == 0) old = __hip_atomic_fetch_add((GAS unsigned*)(ws + WS_C2 + G * 128), 1u, __ATOMIC_RELAXED, __HIP_MEMORY_SCOPE_AGENT);
            if (__builtin_amdgcn_readfirstlane(old) == 31u) ST8(((u32x2){tag, tconv}), rs, lane < 8 ? WS_F2 + (lane * 8 + G) * 128 : WS_END);
        }
        CFENCE();
        if (is_row) {
            pf = GLD(unsigned, pfa, pfo);
            CFENCE();
#pragma unroll
            for (int i = 0; i < 4; ++i) ST16(SENT4, rs, WS_H + bo + i * 1024);
#pragma unroll
            for (int i = 0; i < 4; ++i) LDS(u32x4, L_X + (4 * hw + i) * 1024 + lane * 16) = xr[i];
        } else if (hw == 3) {
            if (f < 12) {
                SPIN {
                    if (none(max4(xr[0], max4(xr[1], max4(xr[2], max4(xr[3], 0u)))) == SENT)) break;
                    __builtin_amdgcn_s_sleep(4);
                    CFENCE();
#pragma unroll
                    for (int i = 0; i < 4; ++i) xr[i] = LD16(rs, bo + i * 1536);
                }
                float s4[4];
#pragma unroll
                for (int e = 0; e < 4; ++e)
                    s4[e] = sadd(sadd(__uint_as_float(xr[0][e]), __uint_as_float(xr[1][e])), sadd(__uint_as_float(xr[2][e]), __uint_as_float(xr[3][e])));
                float s0 = fold2(s4[0], s4[2]); PIN(s0);
                float s1 = fold2(s4[1], s4[3]); PIN(s1);
                s0 = dpp_add<0x118>(s0); s1 = dpp_add<0x118>(s1);
                s0 = sadd(s0, __shfl_xor(s0, 16)); s1 = sadd(s1, __shfl_xor(s1, 16));
                if ((lane & 24) == 8) ST8(((u32x2){pk2(s0, s1), tag}), rs, WS_MOD + (192 * G + 16 * f + 2 * (lane & 7) + (lane >> 5)) * 8);
            }
        } else {
            pf = GLD(unsigned, pfa, pfo);
            CFENCE();
        }
        {
            STAMP(6);
            BAR();
            STAMP(7);
            f32x4 p[2][4];
#pragma unroll
            for (int i = 0; i < 2; ++i) {
                p[i][0] = LDS(f32x4, pr + i * 4608); p[i][1] = LDS(f32x4, pr + i * 4608 + 9216);
                p[i][2] = LDS(f32x4, pr + i * 4608 + 288); p[i][3] = LDS(f32x4, pr + i * 4608 + 9216 + 288);
            }
#pragma unroll
            for (int i = 0; i < 2; ++i) {
                u32x4 r;
#pragma unroll
                for (int e = 0; e < 4; ++e) {
                    const f32x4 pa = p[i][e & 2], pb = p[i][(e & 2) + 1];
                    const f32x2 v = ((f32x2){pa[2 * (e & 1)], pa[2 * (e & 1) + 1]} + (f32x2){kd[i][2 * e], kd[i][2 * e + 1]}) + (f32x2){pb[2 * (e & 1)], pb[2 * (e & 1) + 1]};
                    r[e] = pk2(v[0], v[1]);
                }
                ST16(r, rs, yo + i * 4096);
            }
        }
        CFENCE();
        STAMP(8);
        KEEP(pf);
        asm volatile("s_waitcnt vmcnt(2)" ::: "memory");
    } else {
        const int cw = wave - 4, tc = t - 256;
        const int chl = cw >> 1, h = cw & 1;
        u32x4 A[4][8];
        {
            const auto rk = __builtin_amdgcn_make_buffer_rsrc((void*)(ssmk + (ch0 + chl) * S), 0, S * 2, 0x00020000);
            const unsigned ko = g * 512 + h * 256 + a * 16;
            const u32x4 k0 = __builtin_amdgcn_raw_buffer_load_b128(rk, ko, 0, 0);
            const u32x4 k1 = __builtin_amdgcn_raw_buffer_load_b128(rk, ko + 2048, 0, 0);
            const u32x4 k2 = __builtin_amdgcn_raw_buffer_load_b128(rk, (lane >> 2) * 512 + h * 256 + (lane & 3) * 16 - 32, 0, 0);
            const unsigned wo = (16 * j + (lane >> 2)) * 6144 + 768 * G + 192 * cw + (lane & 3) * 16;
            const u32x2 cv = GLD(u32x2, cin, 32 * j + 8 * g);
            const u32x4 m0 = GLD(u32x4, wmod, wo), m1 = GLD(u32x4, wmod, wo + 64), m2 = GLD(u32x4, wmod, wo + 128);
            CFENCE();
            asm volatile("s_load_dword %0, %1, 0x0" : "=s"(ep) : "s"(ws) : "memory");
            if (cw == 0) LDS(u32x4, L_U + (lane >> 5) * U_STRIDE + (lane & 31) * 16) = (u32x4){0, 0, 0, 0};
            if (cw == 1) { LDS(unsigned, M_CAND + lane * 4) = 0u; LDS(u32x4, M_CNT0) = (u32x4){0, 0, 0, 0}; }
            BAR();
            asm volatile("" : "+s"(ep));
            tag = ep + 1u;
            const unsigned mpo = a < 6 ? WS_MP + (G * 32 + j) * 1536 + cw * 384 + a * 64 + g * 16 : WS_END;
            const unsigned uo = WS_U + (16 * G + (lane >> 2)) * 16384 + j * 512 + cw * 128 + (lane & 3) * 16;
            ST16(SENT4, rs, uo);
            ST16(SENT4, rs, uo + 64);
            ST16(SENT4, rs, mpo);
            if (j == 0 && tc == 64) ST4(0u, rs, WS_C2 + G * 128);
            const unsigned so = is_row ? (tc < 4 ? WS_R + (G * 16 + j) * 128 + tc * 16 : WS_END) : ((f < 7 && tc < 32) ? WS_ACT + (G * 7 + f) * 512 + tc * 16 : WS_END);
            ST16(SENT4, rs, so);
            CFENCE();
            const unsigned kc = L_KC + cw * KCW;
            const unsigned w0 = kc + g * 288 + 32 + a * 16, w2 = kc + (lane >> 2) * 288 + (lane & 3) * 16;
#define SHF(k, n) ((u32x4){__builtin_amdgcn_alignbit(k[1], k[0], 16), __builtin_amdgcn_alignbit(k[2], k[1], 16), __builtin_amdgcn_alignbit(k[3], k[2], 16), __builtin_amdgcn_alignbit(n, k[3], 16)})
#define NXT(k) ((unsigned)__builtin_amdgcn_update_dpp(0, (int)k[0], 0x101, 0xf, 0xf, true))
            LDS(u32x4, w0) = k0; LDS(u32x4, w0 + KC1) = SHF(k0, NXT(k0));
            LDS(u32x4, w0 + 1152) = k1; LDS(u32x4, w0 + 1152 + KC1) = SHF(k1, NXT(k1));
            const u32x4 k2s = SHF(k2, NXT(k2));
            if (lane < 32 && (lane & 2) == 0) { LDS(u32x4, w2) = k2; LDS(u32x4, w2 + KC1) = k2s; }
            const u32x2 sc = {pk2(silu(blo(cv[0])), silu(bhi(cv[0]))), pk2(silu(blo(cv[1])), silu(bhi(cv[1])))};
            const unsigned ml = L_T + cw * 3072;
            LDS(u32x4, ml + (lane >> 2) * 192 + (lane & 3) * 16) = m0;
            LDS(u32x4, ml + (lane >> 2) * 192 + (lane & 3) * 16 + 64) = m1;
            LDS(u32x4, ml + (lane >> 2) * 192 + (lane & 3) * 16 + 128) = m2;
            STAMP(1);
            DRAIN();
            if (lds_arrive<M_CNT0>() == 3u) ST4(tag, rs, lane == 0 ? WS_F1 + (G * 32 + j) * 128 : WS_END);
            STAMP(2);
            f32x4 ms = {0, 0, 0, 0};
#pragma unroll
            for (int i = 0; i < 6; ++i) {
                const f32x4 d = mfma16(trd(ml + (lane >> 2) * 192 + (lane & 3) * 8 + i * 32), sc, (f32x4){0, 0, 0, 0});
                if (a == i) ms = d;
            }
            ST16(__builtin_bit_cast(u32x4, ms), rs, mpo);
            CFENCE();
            STAMP(3);
            const int x0 = 25 + a - 8 * g;
            const int cpy = x0 & 1;
            const unsigned base = kc + cpy * KC1 + (x0 - cpy) * 2;
            const unsigned fo = lane < 8 ? WS_F1 + (G * 32 + 8 * cw + lane) * 128 : WS_END;
#pragma unroll
            for (int mu = 0; mu < 8; ++mu) {
#pragma unroll
                for (int r = 0; r < 4; ++r) {
                    const LAS unsigned* p = (const LAS unsigned*)(base + mu * 288 + r * 64);
                    A[r][mu] = (u32x4){p[0], p[1], p[2], p[3]};
                }
                if (mu == APOLL) {
#pragma unroll
                    for (int m2_ = 0; m2_ <= APOLL; ++m2_)
#pragma unroll
                        for (int r = 0; r < 4; ++r) PIN(A[r][m2_]);
                    SPIN {
                        CFENCE();
                        const unsigned v = LD4(rs, fo);
                        if ((__builtin_amdgcn_ballot_w64(v != tag) & 0xffull) == 0ull) break;
                    }
                    lds_arrive<M_CNT0 + 4>();
                }
            }
#pragma unroll
            for (int r = 0; r < 4; ++r)
#pragma unroll
                for (int mu = APOLL + 1; mu < 8; ++mu) PIN(A[r][mu]);
        }
        unsigned base = L_U + chl * U_STRIDE + U_PADB + (16 * (a - 1) + 8 * g - h * 8 * 16 - 96) * 2;
        unsigned pw = L_P + cw * 9216 + chl * 128 + g * 288 + a * 16;
        PIN(base); PIN(pw);
        f32x4 acc[8];
#pragma unroll
        for (int i = 0; i < 8; ++i) acc[i] = (f32x4){0, 0, 0, 0};
        STAMP(4);
        __builtin_amdgcn_s_setprio(3);
        BAR();
        STAMP(5);
        u32x4 B[2][8];
#pragma unroll
        for (int tau = 0; tau < 8; ++tau) B[0][tau] = LDS(u32x4, base + (256 * tau + 96) * 2);
        SCHED();
#pragma unroll
        for (int r = 0; r < 3; ++r) {
#pragma unroll
            for (int tau = 0; tau < 8; ++tau) {
#pragma unroll
                for (int mu = 0; mu + tau < 8; ++mu) {
                    acc[mu + tau] = mfma(A[r][mu], B[r & 1][tau], acc[mu + tau]);
                    if (tau == 1 || (tau == 2 && mu == 0)) {
                        const int n = tau == 1 ? mu : 7;
                        B[(r + 1) & 1][n] = LDS(u32x4, base + (256 * n + 96 - 32 * (r + 1)) * 2);
                        SCHED();
                    }
                }
                SCHED();
            }
        }
#pragma unroll
        for (int tt = 0; tt < 8; ++tt) {
#pragma unroll
            for (int mu = 0; mu <= tt; ++mu) {
                acc[tt] = mfma(A[3][mu], B[1][tt - mu], acc[tt]);
                if (mu == tt && tt >= 2) LDS(f32x4, pw + (tt - 2) * 1152) = acc[tt - 2];
                if (tt == 7 && mu == 3) LDS(f32x4, pw + 6 * 1152) = acc[6];
                SCHED();
            }
        }
        LDS(f32x4, pw + 7 * 1152) = acc[7];
        __builtin_amdgcn_s_setprio(0);
        STAMP(6);
        BAR();
        STAMP(7);
        if (is_row) {
            const unsigned ww = GLD(unsigned, wr, tc * 4);
            u32x2 m[6];
            SPIN {
                CFENCE();
                unsigned bad = 0;
#pragma unroll
                for (int i = 0; i < 6; ++i) m[i] = LD8(rs, WS_MOD + i * 2048 + tc * 8);
#pragma unroll
                for (int i = 0; i < 6; ++i) bad |= m[i][1] ^ tag;
                if (none(bad != 0u)) break;
                __builtin_amdgcn_s_sleep(8);
            }
            const f32x2 one = {1.f, 1.f};
            const f32x2 ca = b2(m[1][0]) + one, cb = b2(m[4][0]) + one, ga = b2(m[2][0]), sb = b2(m[3][0]), w = b2(ww);
            const f32x2 ca1 = b2(pk2(ca[0], ca[1])), cb1 = b2(pk2(cb[0], cb[1]));
            const f32x2 wc = w * cb1, sw = sb * w;
            LDS(f32x2, L_CH + tc * 8) = ga * ca1;
            LDS(f32x2, L_CH + 2048 + tc * 8) = ga * b2(m[0][0]);
            LDS(f32x2, L_CH + 4096 + tc * 8) = wc;
            LDS(f32x2, L_CH + 6144 + tc * 8) = cb1;
            LDS(f32x2, L_CH + 8192 + tc * 8) = sb;
            LDS(f32x2, L_CH + 10240 + tc * 8) = b2(m[5][0]);
            const float s = half_sum(fold2(wc[0] + wc[1], sw[0] + sw[1]));
            if ((lane & 31) == 31) LDS(float, M_MODP + (lane >> 5) * 16 + cw * 4) = s;
        }
        STAMP(8);
    }
    if (is_row) {
        const int rbk = G * 16 + j;
        const unsigned f2o = lane == 0 ? WS_F2 + (G * 8 + wave) * 128 : WS_END;
        const u32x4 x0 = LDS(u32x4, L_X + wave * 2048 + lane * 16), x1 = LDS(u32x4, L_X + wave * 2048 + 1024 + lane * 16);
        f32x2 nm, rs1;
        {
            float s0 = 0, s1 = 0, q0 = 0, q1 = 0;
#pragma unroll
            for (int e = 0; e < 4; ++e) {
                s0 = dot2(x0[e], 0x3f803f80u, s0); s1 = dot2(x1[e], 0x3f803f80u, s1);
                q0 = dot2(x0[e], x0[e], q0); q1 = dot2(x1[e], x1[e], q1);
            }
            const float mv = half_sum(fold2(s0, s1)) * (1.f / 512.f);
            const float q = half_sum(fold2(q0, q1)) * (1.f / 512.f);
            const float var = rb(fmaxf(q - mv * mv, 0.f));
            const float rstd = __builtin_amdgcn_rcpf(rb(__builtin_amdgcn_sqrtf(rb(var + 1e-5f))));
            const float nmv = -rb(mv) * rstd;
            nm = (f32x2){rdl(nmv, 31), rdl(nmv, 63)}; rs1 = (f32x2){rdl(rstd, 31), rdl(rstd, 63)};
            PINS(nm); PINS(rs1);
        }
        BAR();
        u32x2 fl = LD8(rs, f2o);
        CFENCE();
        STAMP(9);
        f32x2 x[8], gh[8];
        f32x4 wc[2];
        float Wc, Sb;
        {
            const f32x4 w4 = LDS(f32x4, M_MODP), s4 = LDS(f32x4, M_MODP + 16);
#pragma unroll
            for (int hh = 0; hh < 2; ++hh) {
                const f32x4 A1 = LDS(f32x4, L_CH + lane * 32 + hh * 16), A0 = LDS(f32x4, L_CH + 2048 + lane * 32 + hh * 16);
                wc[hh] = LDS(f32x4, L_CH + 4096 + lane * 32 + hh * 16);
#pragma unroll
                for (int e4 = 0; e4 < 4; ++e4) {
                    const int e = hh * 4 + e4;
                    x[e] = (e & 1) ? (f32x2){bhi(x0[e >> 1]), bhi(x1[e >> 1])} : (f32x2){blo(x0[e >> 1]), blo(x1[e >> 1])};
                    gh[e] = pfma(pfma(x[e], rs1, nm), (f32x2){A1[e4], A1[e4]}, (f32x2){A0[e4], A0[e4]});
                    PIN(x[e]); PIN(gh[e]);
                }
                PIN(wc[hh]);
            }
            Wc = (w4[0] + w4[1]) + (w4[2] + w4[3]); Sb = (s4[0] + s4[1]) + (s4[2] + s4[3]);
            PIN(Wc); PIN(Sb);
        }
        unsigned yo = WS_Y + (wave * 32 + (lane >> 2)) * 8192 + rbk * 64 + (lane & 3) * 16;
        unsigned yo2 = yo + 16 * 8192;
        unsigned tpo = L_T + ((lane & 1) * 4 * 516 + wave * 64 + (lane >> 1)) * 4;
        unsigned tro = L_T + wave * 2064 + lane * 32;
        unsigned kb = 0x800u + 2047u - (rbk * 16 + a);
        unsigned ko = M_SUMS + wave * 8 + (lane >> 5) * 4;
        unsigned kr = M_SUMS + a * 4;
        unsigned kg = lane < 16 ? WS_R + rbk * 128 + lane * 4 : WS_END;
        PIN(yo); PIN(yo2); PIN(tpo); PIN(tro); PIN(kb); PIN(ko); PIN(kr); PIN(kg);
        {
            SPIN {
                if (__builtin_amdgcn_readfirstlane(fl[0]) == tag) break;
                CFENCE();
                fl = LD8(rs, f2o);
            }
            wait_until(__builtin_amdgcn_readfirstlane(fl[1]) + D2);
            STAMP(10);
            u32x4 y0, y1;
            SPIN {
                CFENCE();
                y0 = LD16(rs, yo); y1 = LD16(rs, yo2);
                if (none(max4(y0, max4(y1, 0u)) == SENT)) break;
                __builtin_amdgcn_s_sleep(2);
            }
#pragma unroll
            for (int d = 0; d < 4; ++d) { LDS(unsigned, tpo + d * 2064) = y0[d]; LDS(unsigned, tpo + d * 2064 + 128) = y1[d]; }
        }
        STAMP(11);
        BAR();
        float mv, rstd;
        {
            const u32x4 ya = LDS(u32x4, tro), yc = LDS(u32x4, tro + 16);
            f32x2 s1, s2, s3;
#pragma unroll
            for (int e = 0; e < 8; ++e) {
                x[e] = pfma(gh[e], b2(e < 4 ? ya[e & 3] : yc[e & 3]), x[e]);
                const float w = wc[e >> 2][e & 3];
                if (e == 0) { s1 = x[0]; s2 = x[0] * x[0]; s3 = x[0] * w; }
                else { s1 += x[e]; s2 = pfma(x[e], x[e], s2); s3 = pfma(x[e], (f32x2){w, w}, s3); }
            }
            const float v1 = half_sum(fold2(s1[0], s1[1])), v2 = half_sum(fold2(s2[0], s2[1])), v3 = half_sum(fold2(s3[0], s3[1]));
            if ((lane & 31) == 31) { LDS(float, ko) = v1; LDS(float, ko + 64) = v2; LDS(float, ko + 128) = v3; }
        }
        BAR();
        {
            mv = LDS(float, kr) * (1.f / 512.f);
            const float q = LDS(float, kr + 64) * (1.f / 512.f);
            const float s = LDS(float, kr + 128);
            const float var = rb(fmaxf(q - mv * mv, 0.f));
            rstd = __builtin_amdgcn_rcpf(rb(__builtin_amdgcn_sqrtf(rb(var + 1e-5f))));
            if (wave == 0) {
                const float r = rstd * (s - mv * Wc) + Sb;
                const int u = (int)(pk2(r, r) & 0xffff0000u);
                const unsigned ord = ((unsigned)u ^ ((unsigned)(u >> 31) | 0x80000000u)) >> 16;
                ST4((ord << 12) + kb, rs, kg);
            }
        }
        CFENCE();
        STAMP(12);
        const auto ro = __builtin_amdgcn_make_buffer_rsrc(out, 0, S * D * 2, 0x00020000);
        {
            const float nmv = -mv * rstd;
            const f32x2 r2 = {rdl(rstd, 2 * wave), rdl(rstd, 2 * wave + 1)}, n2 = {rdl(nmv, 2 * wave), rdl(nmv, 2 * wave + 1)};
            f32x2 h2[8];
            f32x4 gb[2];
#pragma unroll
            for (int hh = 0; hh < 2; ++hh) {
                const f32x4 cb1 = LDS(f32x4, L_CH + 6144 + lane * 32 + hh * 16), sb = LDS(f32x4, L_CH + 8192 + lane * 32 + hh * 16);
                gb[hh] = LDS(f32x4, L_CH + 10240 + lane * 32 + hh * 16);
#pragma unroll
                for (int e4 = 0; e4 < 4; ++e4)
                    h2[hh * 4 + e4] = pfma(pfma(x[hh * 4 + e4], r2, n2), (f32x2){cb1[e4], cb1[e4]}, (f32x2){sb[e4], sb[e4]});
            }
            const unsigned oo = (rbk * 16 + 2 * wave) * 1024 + lane * 16;
#pragma unroll
            for (int i = 0; i < 2; ++i)
                ST16(((u32x4){pk2(h2[0][i], h2[1][i]), pk2(h2[2][i], h2[3][i]), pk2(h2[4][i], h2[5][i]), pk2(h2[6][i], h2[7][i])}), rs, WS_H + oo + i * 1024);
#pragma unroll
            for (int e = 0; e < 8; ++e) { const float gv = gb[e >> 2][e & 3]; x[e] = pfma(h2[e], (f32x2){gv, gv}, x[e]); }
#pragma unroll
            for (int i = 0; i < 2; ++i)
                ST16(((u32x4){pk2(x[0][i], x[1][i]), pk2(x[2][i], x[3][i]), pk2(x[4][i], x[5][i]), pk2(x[6][i], x[7][i])}), ro, oo + i * 1024);
        }
        DRAIN();
        BAR();
        if (t == 0) ST4(tag, rs, WS_F3 + rbk * 128);
        STAMP(13);
    } else {
        if (f >= 11) return;
        const bool is_gu = f < 7;
        u32x4 Wf[8];
        {
            const unsigned wl = L_W + wave * 8192;
            if (is_gu) {
                const u16* wp = wave < 4 ? wg : wu;
                const unsigned wo = ((wave & 3) * 128 + (lane >> 2)) * (F * 2) + (224 * G + 32 * f + 8 * (lane & 3)) * 2;
                u32x4 W[8];
#pragma unroll
                for (int i = 0; i < 8; ++i) W[i] = GLD(u32x4, wp, wo + i * 16 * (F * 2));
#pragma unroll
                for (int i = 0; i < 8; ++i) LDS(u32x4, wl + i * 1024 + lane * 16) = W[i];
                const unsigned rp = wl + (8 * g + (a >> 2)) * 64 + (lane & 3) * 8;
#pragma unroll
                for (int c = 0; c < 2; ++c)
#pragma unroll
                    for (int ks = 0; ks < 4; ++ks) Wf[c * 4 + ks] = trd2(rp + ks * 2048 + c * 32, 256);
            } else {
                const unsigned wo = (wave * 224 + (lane >> 1)) * (D * 2) + (64 * G + 16 * (f - 7) + 8 * (lane & 1)) * 2;
                u32x4 W[7];
#pragma unroll
                for (int i = 0; i < 7; ++i) W[i] = GLD(u32x4, wd, wo + i * 32 * (D * 2));
#pragma unroll
                for (int i = 0; i < 7; ++i) LDS(u32x4, wl + i * 1024 + lane * 16) = W[i];
                const unsigned rp = wl + (8 * g + (a >> 2)) * 32 + (lane & 3) * 8;
#pragma unroll
                for (int ks = 0; ks < 7; ++ks) Wf[ks] = trd2(rp + ks * 1024, 128);
                Wf[7] = Wf[0];
            }
#pragma unroll
            for (int i = 0; i < 8; ++i) PIN(Wf[i]);
        }
        STAMP(9);
        unsigned ro_ = WS_R + (t >> 2) * 128 + (t & 3) * 16, cko = M_CAND + (t >> 3) * 4, cco = M_CAND + (t & 7) * 32, xw = L_X + wave * 1040 + lane * 16;
        unsigned hb = WS_H + 2047 * 1024 + 0x800 * 1024 + lane * 16, xrd = L_X + (a & 7) * 1040 + (wave & 3) * 256 + g * 16;
        unsigned pwo = is_gu ? L_P + (wave * 8 + a) * 144 + g * 16 : L_P + (wave * 8 + a) * 80 + g * 16;
        PIN(ro_); PIN(cko); PIN(cco); PIN(xw); PIN(xrd); PIN(hb); PIN(pwo);
        {
            u32x2 fl;
            SPIN {
                CFENCE();
                fl = LD8(rs, lane < 8 ? WS_F2 + (G * 8 + lane) * 128 : WS_END);
                if ((__builtin_amdgcn_ballot_w64(fl[0] != tag) & 0xffull) == 0ull) break;
                __builtin_amdgcn_s_sleep(4);
            }
            const unsigned now = rt32();
            const unsigned tl = wave_max(lane < 8 ? fl[1] - now + 0x40000000u : 0u) - 0x40000000u + now;
            wait_until(tl + D3);
        }
        STAMP(10);
        {
            u32x4 key;
            unsigned lm;
            SPIN {
                CFENCE();
                key = LD16(rs, ro_);
                lm = max4(key, 0u);
                if (none(lm == SENT)) break;
            }
            STAMP(11);
            const unsigned wm_ = wave_max_v(lm);
            if (lane == 63) LDS(unsigned, M_WMAX + wave * 4) = wm_;
            const unsigned a0 = max(key[0], key[1]), a1 = min(key[0], key[1]), a2 = max(key[2], key[3]), a3 = min(key[2], key[3]);
            const unsigned m1 = min(a0, a2), m2 = max(a1, a3);
            unsigned k0 = lm, k1 = max(m1, m2), k2 = min(m1, m2), k3 = min(a1, a3);
            BAR();
            const u32x4 ma = LDS(u32x4, M_WMAX), mb = LDS(u32x4, M_WMAX + 16);
            unsigned T = min(min(min(ma[0], ma[1]), min(ma[2], ma[3])), min(min(mb[0], mb[1]), min(mb[2], mb[3])));
            const u64 b0 = __builtin_amdgcn_ballot_w64(k0 >= T), b1 = __builtin_amdgcn_ballot_w64(k1 >= T);
            const int n0 = __builtin_popcountll(b0);
            if (__builtin_amdgcn_ballot_w64(k2 >= T) == 0ull && n0 + __builtin_popcountll(b1) <= 8) {
                const int pos = __builtin_amdgcn_mbcnt_hi((unsigned)(b0 >> 32), __builtin_amdgcn_mbcnt_lo((unsigned)b0, wave * 8));
                if (k0 >= T) LDS(unsigned, M_CAND + pos * 4) = k0;
                if (b1 != 0ull) {
                    const int pos1 = __builtin_amdgcn_mbcnt_hi((unsigned)(b1 >> 32), __builtin_amdgcn_mbcnt_lo((unsigned)b1, wave * 8 + n0));
                    if (k1 >= T) LDS(unsigned, M_CAND + pos1 * 4) = k1;
                }
            } else {
#pragma unroll 1
                for (int i = 0; i < 8; ++i) {
                    const unsigned mx = wave_max(k0);
                    if (mx < __builtin_amdgcn_readfirstlane(T)) break;
                    if (k0 == mx) { LDS(unsigned, M_CAND + (wave * 8 + i) * 4) = mx; k0 = k1; k1 = k2; k2 = k3; k3 = 0u; }
                }
            }
            BAR();
            {
                const unsigned kx = LDS(unsigned, cko);
                const u32x4 c0 = LDS(u32x4, cco), c1 = LDS(u32x4, cco + 16);
                unsigned bits = 0;
#pragma unroll
                for (int e = 0; e < 4; ++e) bits = __builtin_amdgcn_alignbit(bits, kx - c0[e], 31);
#pragma unroll
                for (int e = 0; e < 4; ++e) bits = __builtin_amdgcn_alignbit(bits, kx - c1[e], 31);
                int rank = __builtin_popcount(bits);
                rank += __builtin_amdgcn_update_dpp(0, rank, 0xb1, 0xf, 0xf, true);
                rank += __builtin_amdgcn_update_dpp(0, rank, 0x4e, 0xf, 0xf, true);
                rank += __builtin_amdgcn_update_dpp(0, rank, 0x141, 0xf, 0xf, true);
                if (rank < 8) LDS(unsigned, M_SEL + rank * 4) = kx;
            }
            BAR();
        }
        STAMP(12);
        if (is_gu) {
            const unsigned ho = hb - ((LDS(unsigned, M_SEL + wave * 4) & 0xfffu) << 10);
            u32x4 hv;
            SPIN {
                CFENCE();
                hv = LD16(rs, ho);
                if (none(max4(hv, 0u) == SENT)) break;
            }
            LDS(u32x4, xw) = hv;
            STAMP(13);
            BAR();
            u32x4 xa[4];
#pragma unroll
            for (int ks = 0; ks < 4; ++ks) xa[ks] = LDS(u32x4, xrd + ks * 64);
            f32x4 acc0 = {0, 0, 0, 0}, acc1 = {0, 0, 0, 0};
#pragma unroll
            for (int ks = 0; ks < 4; ++ks) { acc0 = mfma(Wf[ks], xa[ks], acc0); acc1 = mfma(Wf[4 + ks], xa[ks], acc1); }
            if (a < 8) { LDS(f32x4, pwo) = acc0; LDS(f32x4, pwo + 64) = acc1; }
            BAR();
            if (wave < 4) {
                const unsigned pp = L_P + (lane >> 3) * 144 + (wave * 8 + (lane & 7)) * 4;
                float v[8];
#pragma unroll
                for (int w = 0; w < 8; ++w) v[w] = LDS(float, pp + w * 1152);
                const float gp = rb((v[0] + v[1]) + (v[2] + v[3])), up = rb((v[4] + v[5]) + (v[6] + v[7]));
                const float o = rb(silu(gp)) * up;
                const float on = __int_as_float(__builtin_amdgcn_update_dpp(0, __float_as_int(o), 0xb1, 0xf, 0xf, true));
                ST4(pk2(o, on), rs, (t & 1) ? WS_END : WS_ACT + (G * 7 + f) * 512 + t * 2);
            }
            STAMP(14);
        } else {
            const unsigned tk = rt32();
            const auto ro = __builtin_amdgcn_make_buffer_rsrc(out, 0, S * D * 2, 0x00020000);
            const int col0 = 64 * G + 16 * (f - 7);
            float o0 = 0, o1 = 0, g0 = 0, g1 = 0;
            unsigned ooff = 0;
            if (wave == 0) {
                const unsigned fk = LDS(unsigned, M_SEL + (lane >> 3) * 4);
                const int frow = 2047 - (int)(fk & 0x7ffu);
                unsigned u = (fk >> 12) ^ 0x8000u;
                u = (u & 0x8000u) ? (u ^ 0x7fffu) : u;
                const float sig = rb(__builtin_amdgcn_rcpf(1.f + __expf(-blo(u))));
                u32x2 gbv;
                SPIN {
                    CFENCE();
                    gbv = LD8(rs, WS_MOD + (1280 + col0 / 2 + (lane & 7)) * 8);
                    const unsigned v = LD4(rs, WS_F3 + (frow >> 4) * 128);
                    if (none(((v ^ tag) | (gbv[1] ^ tag)) != 0u)) break;
                    __builtin_amdgcn_s_sleep(2);
                }
                CFENCE();
                ooff = (frow * D + col0 + 2 * (lane & 7)) * 2;
                const unsigned oprev = LD4(ro, ooff);
                o0 = blo(oprev); o1 = bhi(oprev); g0 = blo(gbv[0]) * sig; g1 = bhi(gbv[0]) * sig;
                PIN(o0); PIN(o1); PIN(g0); PIN(g1);
            }
            unsigned ao = a < 8 ? WS_ACT + wave * 7 * 512 + g * 128 + a * 16 : WS_END;
            unsigned pp = L_P + (lane >> 3) * 80 + (lane & 7) * 8;
            PIN(ao); PIN(pp);
            wait_until(tk + D4);
            u32x4 av[7];
            SPIN {
                CFENCE();
#pragma unroll
                for (int ks = 0; ks < 7; ++ks) av[ks] = LD16(rs, ao + ks * 512);
                unsigned m = 0;
#pragma unroll
                for (int ks = 0; ks < 7; ++ks) m = max4(av[ks], m);
                if (none(m == SENT)) break;
                __builtin_amdgcn_s_sleep(1);
            }
            STAMP(13);
            f32x4 acc0 = {0, 0, 0, 0};
#pragma unroll
            for (int ks = 0; ks < 7; ++ks) acc0 = mfma(Wf[ks], av[ks], acc0);
            if (a < 8) LDS(f32x4, pwo) = acc0;
            BAR();
            if (wave == 0) {
                f32x2 v[8];
#pragma unroll
                for (int w = 0; w < 8; ++w) v[w] = LDS(f32x2, pp + w * 640);
                const f32x2 s = ((v[0] + v[1]) + (v[2] + v[3])) + ((v[4] + v[5]) + (v[6] + v[7]));
                ST4(pk2(o0 + g0 * rb(s[0]), o1 + g1 * rb(s[1])), ro, ooff);
                if (G == 0 && f == 7 && lane == 0) ST4(tag, rs, WS_E);
            }
            STAMP(14);
        }
    }
}

extern "C" void kernel_launch(void* const* d_in, const int* in_sizes, int n_in,
                              void* d_out, int out_size, void* d_ws, size_t ws_size,
                              hipStream_t stream) {
    fused_kernel<<<256, 512, 0, stream>>>((const u16*)d_in[1], (const u16*)d_in[2], (char*)d_ws, (const u16*)d_in[4],
                                          (const u16*)d_in[3], (const u16*)d_in[11], (const u16*)d_in[12], (const u16*)d_in[5],
                                          (const u16*)d_in[0], (const u16*)d_in[7], (const u16*)d_in[8], (const u16*)d_in[9],
                                          (const u16*)d_in[10], (u16*)d_out);
}
```

```cpp
#define PROBE 99
#define PG 0
#include <hip/hip_runtime.h>
#include <stdint.h>
#pragma clang diagnostic ignored "-Wint-to-pointer-cast"

typedef unsigned short u16;
typedef unsigned long long u64;
typedef short bf16x8 __attribute__((ext_vector_type(8)));
typedef short s16x4 __attribute__((ext_vector_type(4)));
typedef __bf16 bf2 __attribute__((ext_vector_type(2)));
typedef float f32x4 __attribute__((ext_vector_type(4)));
typedef float f32x2 __attribute__((ext_vector_type(2)));
typedef unsigned u32x4 __attribute__((ext_vector_type(4)));
typedef unsigned u32x2 __attribute__((ext_vector_type(2)));
#define LAS __attribute__((address_space(3)))
#define GAS __attribute__((address_space(1)))

constexpr int S = 2048, D = 512, F = 1792;
constexpr unsigned SENT = 0xFFFFFFFFu;

constexpr unsigned WS_E = 0;
constexpr unsigned WS_C2 = 1024;
constexpr unsigned WS_F1 = 2048;
constexpr unsigned WS_F2 = WS_F1 + 256 * 128;
constexpr unsigned WS_F3 = WS_F2 + 64 * 128;
constexpr unsigned WS_MOD = WS_F3 + 128 * 128;
constexpr unsigned WS_R = WS_MOD + 12288;
constexpr unsigned WS_ACT = WS_R + 16384;
constexpr unsigned WS_MP = 131072;
constexpr unsigned WS_U = WS_MP + 393216;
constexpr unsigned WS_Y = WS_U + 2097152;
constexpr unsigned WS_H = WS_Y + 2097152;
constexpr unsigned WS_END = WS_H + 2097152;
constexpr unsigned WS_DBG = WS_END;
static_assert(WS_ACT + 28672 <= WS_MP, "ws layout");

constexpr int U_PADB = 512, U_STRIDE = 4672;
constexpr int L_U = 0;
constexpr int L_P = 9344;
constexpr int L_MISC = L_P + 4 * 9216 + 128;
constexpr int L_S = L_MISC + 2048;
constexpr int KROW = 192, SW = 36 * KROW;
constexpr int KC1 = 2368, KCW = 4736;
constexpr int L_KC = L_S + 32768;
constexpr int L_T = L_KC + 4 * KCW;
constexpr int L_CH = L_T + 8 * 2064;
constexpr int L_X = L_CH + 12288;
constexpr int L_W = L_S;
constexpr int L_END = L_X + 16384;
static_assert(L_W + 65536 <= L_CH, "lds layout");
static_assert(L_END <= 160 * 1024 && L_END > 80 * 1024, "lds size");
constexpr int M_WMAX = L_MISC;
constexpr int M_SEL = L_MISC + 64;
constexpr int M_CNT0 = L_MISC + 96;
constexpr int M_SUMS = L_MISC + 128;
constexpr int M_CAND = L_MISC + 768;
constexpr int M_MODP = L_MISC + 512;

#define APOLL 3
#define D2 185
#define D3 0
#define D4 0
constexpr unsigned SPIN_CAP = 40000;

__device__ __forceinline__ float blo(unsigned w) { return __uint_as_float(w << 16); }
__device__ __forceinline__ float bhi(unsigned w) { return __uint_as_float(w & 0xffff0000u); }
__device__ __forceinline__ f32x2 b2(unsigned w) { return (f32x2){blo(w), bhi(w)}; }
__device__ __forceinline__ float rb(float f) { return (float)(__bf16)f; }
__device__ __forceinline__ unsigned pk2(float lo, float hi) {
    bf2 v; v[0] = (__bf16)lo; v[1] = (__bf16)hi;
    return __builtin_bit_cast(unsigned, v);
}
__device__ __forceinline__ float dot2(unsigned a, unsigned b, float c) {
    return __builtin_amdgcn_fdot2_f32_bf16(__builtin_bit_cast(bf2, a), __builtin_bit_cast(bf2, b), c, false);
}
__device__ __forceinline__ f32x2 pfma(f32x2 a, f32x2 b, f32x2 c) { return __builtin_elementwise_fma(a, b, c); }
__device__ __forceinline__ unsigned rt32() { return (unsigned)__builtin_amdgcn_s_memrealtime(); }
#define BAR() do { asm volatile("s_waitcnt lgkmcnt(0)" ::: "memory"); __builtin_amdgcn_s_barrier(); asm volatile("" ::: "memory"); } while (0)
#define DRAIN() asm volatile("s_waitcnt vmcnt(0)" ::: "memory")
#define CFENCE() asm volatile("" ::: "memory")
#define PIN(x) asm volatile("" : "+v"(x))
#define PINS(x) asm volatile("" : "+s"(x))
#define KEEP(x) asm volatile("" :: "v"(x))
#define SCHED() __builtin_amdgcn_sched_barrier(0)
#define SPIN _Pragma("unroll 1") for (unsigned sp_ = 0; sp_ < SPIN_CAP; ++sp_)
#define LDS(T, off) (*(LAS T*)(unsigned)(off))
template <int OFF>
__device__ __forceinline__ unsigned lds_arrive() {
    unsigned old; u64 ex;
    asm volatile("s_mov_b64 %1, exec\n\ts_mov_b64 exec, 1\n\tds_add_rtn_u32 %0, %2, %3 offset:%4\n\ts_mov_b64 exec, %1\n\ts_waitcnt lgkmcnt(0)"
                 : "=&v"(old), "=&s"(ex) : "v"(0u), "v"(1u), "i"(OFF) : "memory");
    return __builtin_amdgcn_readfirstlane(old);
}
template <int OFF>
__device__ __forceinline__ void lds_wait(unsigned want) {
#pragma unroll 1
    for (unsigned sp_ = 0; sp_ < 4000000u; ++sp_) { CFENCE(); if (__builtin_amdgcn_readfirstlane(LDS(volatile unsigned, OFF)) >= want) break; }
    CFENCE();
}
__device__ __forceinline__ float smul(float a, float b) { float r; asm("v_mul_f32 %0, %1, %2" : "=v"(r) : "v"(a), "v"(b)); return r; }
__device__ __forceinline__ float sadd(float a, float b) { float r; asm("v_add_f32 %0, %1, %2" : "=v"(r) : "v"(a), "v"(b)); return r; }

template <int CTRL, int RMASK = 0xf>
__device__ __forceinline__ float dpp_add(float v) {
    return v + __int_as_float(__builtin_amdgcn_update_dpp(0, __float_as_int(v), CTRL, RMASK, 0xf, true));
}
__device__ __forceinline__ float half_sum(float v) {
    v = dpp_add<0x111>(v); v = dpp_add<0x112>(v); v = dpp_add<0x114>(v); v = dpp_add<0x118>(v);
    return dpp_add<0x142>(v);
}
__device__ __forceinline__ float fold2(float a, float b) {
    auto r = __builtin_amdgcn_permlane32_swap(__float_as_uint(a), __float_as_uint(b), false, false);
    return __uint_as_float(r[0]) + __uint_as_float(r[1]);
}
__device__ __forceinline__ float rdl(float v, int l) { return __int_as_float(__builtin_amdgcn_readlane(__float_as_int(v), l)); }
template <int CTRL, int RMASK = 0xf>
__device__ __forceinline__ unsigned dpp_max(unsigned v) {
    return max(v, (unsigned)__builtin_amdgcn_update_dpp(0, (int)v, CTRL, RMASK, 0xf, true));
}
__device__ __forceinline__ unsigned wave_max_v(unsigned v) {
    v = dpp_max<0x111>(v); v = dpp_max<0x112>(v); v = dpp_max<0x114>(v); v = dpp_max<0x118>(v);
    v = dpp_max<0x142>(v); v = dpp_max<0x143>(v);
    return v;
}
__device__ __forceinline__ unsigned wave_max(unsigned v) { return (unsigned)__builtin_amdgcn_readlane((int)wave_max_v(v), 63); }
__device__ __forceinline__ u32x2 trd(unsigned off) {
    return __builtin_bit_cast(u32x2, __builtin_amdgcn_ds_read_tr16_b64_v4i16((LAS s16x4*)off));
}
__device__ __forceinline__ u32x4 trd2(unsigned off, unsigned d) { const u32x2 lo = trd(off), hi = trd(off + d); return (u32x4){lo[0], lo[1], hi[0], hi[1]}; }
__device__ __forceinline__ f32x4 mfma(u32x4 a, u32x4 b, f32x4 c) {
    return __builtin_amdgcn_mfma_f32_16x16x32_bf16(__builtin_bit_cast(bf16x8, a), __builtin_bit_cast(bf16x8, b), c, 0, 0, 0);
}
__device__ __forceinline__ f32x4 mfma16(u32x2 a, u32x2 b, f32x4 c) {
    return __builtin_amdgcn_mfma_f32_16x16x16bf16_1k(__builtin_bit_cast(s16x4, a), __builtin_bit_cast(s16x4, b), c, 0, 0, 0);
}
__device__ __forceinline__ float silu(float x) { return x * __builtin_amdgcn_rcpf(1.f + __expf(-x)); }
__device__ __forceinline__ unsigned max3u(unsigned a, unsigned b, unsigned c) { return max(max(a, b), c); }
__device__ __forceinline__ unsigned max4(u32x4 v, unsigned m) { return max3u(max3u(v[0], v[1], m), v[2], v[3]); }
__device__ __forceinline__ bool none(bool c) { return __builtin_amdgcn_ballot_w64(c) == 0ull; }
__device__ __forceinline__ void wait_until(unsigned t) {
    const unsigned now = rt32();
    if ((int)(t - now) > 400) t = now + 400;
#pragma unroll 1
    while ((int)(rt32() - t) < 0) {}
}

#define STAMP(i) do { if ((i) == 0) { pt1 = rt32(); asm volatile("s_waitcnt lgkmcnt(0)" : "+s"(ptA)); pt0 = (unsigned)ptA; } if ((i) == 14 && wave == 0 && f == 7 && G == PG) { const unsigned e_ = rt32() + 8u * (pt1 - pt0); while ((int)(rt32() - e_) < 0) {} } } while (0)
#define NST 0

#define LD16(rs, off) __builtin_amdgcn_raw_buffer_load_b128(rs, off, 0, 16)
#define LD8(rs, off) __builtin_amdgcn_raw_buffer_load_b64(rs, off, 0, 16)
#define LD4(rs, off) __builtin_amdgcn_raw_buffer_load_b32(rs, off, 0, 16)
#define ST16(v, rs, off) __builtin_amdgcn_raw_buffer_store_b128(v, rs, off, 0, 16)
#define ST8(v, rs, off) __builtin_amdgcn_raw_buffer_store_b64(v, rs, off, 0, 16)
#define ST4(v, rs, off) __builtin_amdgcn_raw_buffer_store_b32(v, rs, off, 0, 16)
#define SENT4 ((u32x4){SENT, SENT, SENT, SENT})
#define GLD(T, p, off) (*(const GAS T*)((const GAS char*)(p) + (off)))

__global__ void __launch_bounds__(512, 2)
fused_kernel(const u16* __restrict__ kin, const u16* __restrict__ vin, char* __restrict__ ws, const u16* __restrict__ ssmk,
             const u16* __restrict__ shk, const u16* __restrict__ cin, const u16* __restrict__ wmod, const u16* __restrict__ ssmD,
             const u16* __restrict__ xq, const u16* __restrict__ wr, const u16* __restrict__ wg, const u16* __restrict__ wu,
             const u16* __restrict__ wd, u16* __restrict__ out) {
    __shared__ __attribute__((aligned(16))) char lds_[L_END];
    u64 ptA; asm volatile("s_memrealtime %0" : "=s"(ptA));
    asm volatile("" :: "v"((LAS char*)lds_) : "memory");
    const int b = blockIdx.x, t = threadIdx.x, lane = t & 63;
    const int wave = __builtin_amdgcn_readfirstlane(t >> 6);
    const int G = b & 7, j = b >> 3;
    const int ch0 = 64 * G + 2 * j;
    PINS(kin); PINS(vin); PINS(ws); PINS(ssmk); PINS(shk); PINS(cin); PINS(wmod); PINS(ssmD);
    PINS(xq); PINS(wr); PINS(wg); PINS(wu); PINS(wd); PINS(out);
    const auto rs = __builtin_amdgcn_make_buffer_rsrc(ws, 0, WS_END, 0x00020000);
    const bool is_row = j < 16;
    const int f = j - 16;
    unsigned pt0 = 0, pt1 = 0;
    STAMP(0);
    const unsigned yb = WS_Y + (G * 32 + j) * 8192;
    const int a = lane & 15, g = lane >> 4;
    unsigned ep, dd;
    unsigned tag;
    if (wave < 4) {
        const int hw = wave;
        {
            const unsigned toff = ((64 * j + 16 * hw + (lane >> 3)) * D + 64 * G + 8 * (lane & 7)) * 2;
            const u32x4 ka = GLD(u32x4, kin, toff), va = GLD(u32x4, vin, toff);
            const u32x4 kb = GLD(u32x4, kin, toff + 8192), vb = GLD(u32x4, vin, toff + 8192);
            u32x4 kh = {0, 0, 0, 0};
            if (lane < 32 && toff >= 4096u) kh = GLD(u32x4, kin, toff - 4096u);
            const u32x2 shw = GLD(u32x2, shk, (64 * G + lane) * 8);
            CFENCE();
            asm volatile("s_load_dword %0, %2, 0x0\n\ts_load_dword %1, %3, 0x0" : "=&s"(ep), "=&s"(dd) : "s"(ws), "s"(ssmD + ch0) : "memory");
            unsigned uo = WS_U + (16 * G + (lane >> 2)) * 16384 + j * 512 + hw * 128 + (lane & 3) * 16;
            const int q = a >> 2;
            unsigned sw = L_S + hw * SW + (lane >> 3) * KROW + (lane & 7) * 16;
            unsigned ra = L_S + hw * SW + q * KROW + 32 * g + 8 * (lane & 3);
            unsigned rb_ = ra + (17 - (q >> 1)) * KROW;
            f32x2 o[8];
#pragma unroll
            for (int i = 0; i < 8; ++i) { o[i] = (f32x2){0.f, 0.f}; PIN(o[i]); }
            PIN(sw); PIN(ra); PIN(rb_); PIN(uo);
            BAR();
            asm volatile("" : "+s"(ep), "+s"(dd));
            tag = ep + 1u;
            LDS(u32x4, sw + 4 * KROW) = ka; LDS(u32x4, sw + 20 * KROW) = va;
            LDS(u32x4, sw + 12 * KROW) = kb; LDS(u32x4, sw + 28 * KROW) = vb;
            if (lane < 32) LDS(u32x4, sw) = kh;
            CFENCE();
            STAMP(1);
            const unsigned w32 = __builtin_amdgcn_alignbit(shw[1], shw[1], 16), w10 = __builtin_amdgcn_alignbit(shw[0], shw[0], 16);
            unsigned Q[18], V[8];
#pragma unroll
            for (int i = 0; i < 4; ++i) {
                const u32x2 qo = trd(ra + (1 + 4 * i) * KROW), qe = trd(ra + (2 + 4 * i) * KROW), vv = trd(ra + (20 + 4 * i) * KROW);
                Q[4 * i] = qo[0]; Q[4 * i + 2] = qo[1]; Q[4 * i + 1] = qe[0]; Q[4 * i + 3] = qe[1]; V[2 * i] = vv[0]; V[2 * i + 1] = vv[1];
            }
            { const u32x2 ql = trd(rb_); Q[16] = ql[0]; Q[17] = ql[1]; }
            const unsigned cl = LDS(volatile unsigned, M_CNT0);
#pragma unroll
            for (int hh = 0; hh < 2; ++hh) {
#pragma unroll
                for (int i = 8 * hh; i < 8 * hh + 8; ++i) o[i >> 1][i & 1] = dot2(Q[i + 2], w10, dot2(Q[i], w32, o[i >> 1][i & 1]));
#pragma unroll
                for (int i = 4 * hh; i < 4 * hh + 4; ++i) o[i] *= b2(V[i]);
                u32x4 r = {pk2(o[4 * hh + 3][1], o[4 * hh + 3][0]), pk2(o[4 * hh + 2][1], o[4 * hh + 2][0]), pk2(o[4 * hh + 1][1], o[4 * hh + 1][0]), pk2(o[4 * hh][1], o[4 * hh][0])};
                if (hh == 0) { PIN(r); if (__builtin_amdgcn_readfirstlane(cl) < 4u) lds_wait<M_CNT0>(4u); }
                ST16(r, rs, uo + 64 * hh);
                SCHED();
            }
            CFENCE();
            STAMP(2);
        }
        unsigned ub = WS_U + (16 * G + (j >> 1)) * 16384 + (j & 1) * 32 + (hw * 64 + (lane >> 1)) * 64 + (lane & 1) * 16;
        unsigned ul = L_U + (lane & 1) * U_STRIDE + U_PADB + (hw * 64 + (lane >> 1)) * 16;
        const u16* pfa = hw == 0 ? wg : hw == 1 ? wu : wd;
        unsigned pfo = hw < 2 ? (16 * j + (lane >> 2)) * (F * 2) + ((448 * G) / 128 + (lane & 3)) * 128
                              : (56 * j + min(lane, 55)) * (D * 2) + 128 * G;
        unsigned bo = is_row ? ((G * 16 + j) * 16 + 4 * hw) * 1024 + lane * 16
                             : WS_MP + (G * 32 + 4 * (lane >> 3)) * 1536 + f * 128 + (lane & 7) * 16;
        const int chl = (t >> 1) & 1, half = t & 1;
        unsigned pr = L_P + chl * (2 * 9216 + 128) + hw * 1152 + half * 576 + (lane >> 2) * 16;
        unsigned yo = yb + t * 16;
        unsigned kvo = L_U + chl * U_STRIDE + U_PADB + (t >> 2) * 32 + half * 16;
        float Dv = chl ? bhi(dd) : blo(dd);
        PIN(ub); PIN(ul); PIN(pfo); PIN(bo); PIN(pr); PIN(yo); PIN(kvo); PIN(Dv);
        DRAIN();
        STAMP(3);
        lds_wait<M_CNT0 + 4>(4u);
        STAMP(4);
        u32x4 u0, u1;
        SPIN {
            CFENCE();
            u0 = LD16(rs, ub); u1 = LD16(rs, ub + 2048);
            if (none(max4(u0, max4(u1, 0u)) == SENT)) break;
        }
        LDS(u32x4, ul) = u0; LDS(u32x4, ul + 512) = u1;
        BAR();
        const unsigned tconv = rt32();
        STAMP(5);
        unsigned pf = 0;
        u32x4 xr[4];
        if (is_row) {
#pragma unroll
            for (int i = 0; i < 4; ++i) xr[i] = GLD(u32x4, xq, bo + i * 1024);
        } else if (hw == 3 && f < 12) {
#pragma unroll
            for (int i = 0; i < 4; ++i) xr[i] = LD16(rs, bo + i * 1536);
        }
        CFENCE();
        ST16(SENT4, rs, yo); ST16(SENT4, rs, yo + 4096);
        CFENCE();
        float kd[2][8];
#pragma unroll
        for (int i = 0; i < 2; ++i) {
            const u32x4 kv = LDS(u32x4, kvo + i * 2048);
#pragma unroll
            for (int e = 0; e < 4; ++e) { kd[i][2 * e] = smul(bhi(kv[3 - e]), Dv); kd[i][2 * e + 1] = smul(blo(kv[3 - e]), Dv); PIN(kd[i][2 * e]); PIN(kd[i][2 * e + 1]); }
        }
        DRAIN();
        if (lds_arrive<M_CNT0 + 8>() == 3u) {
            unsigned old = 0;
            if (lane == 0) old = __hip_atomic_fetch_add((GAS unsigned*)(ws + WS_C2 + G * 128), 1u, __ATOMIC_RELAXED, __HIP_MEMORY_SCOPE_AGENT);
            if (__builtin_amdgcn_readfirstlane(old) == 31u) ST8(((u32x2){tag, tconv}), rs, lane < 8 ? WS_F2 + (lane * 8 + G) * 128 : WS_END);
        }
        CFENCE();
        if (is_row) {
            pf = GLD(unsigned, pfa, pfo);
            CFENCE();
#pragma unroll
            for (int i = 0; i < 4; ++i) ST16(SENT4, rs, WS_H + bo + i * 1024);
#pragma unroll
            for (int i = 0; i < 4; ++i) LDS(u32x4, L_X + (4 * hw + i) * 1024 + lane * 16) = xr[i];
        } else if (hw == 3) {
            if (f < 12) {
                SPIN {
                    if (none(max4(xr[0], max4(xr[1], max4(xr[2], max4(xr[3], 0u)))) == SENT)) break;
                    __builtin_amdgcn_s_sleep(4);
                    CFENCE();
#pragma unroll
                    for (int i = 0; i < 4; ++i) xr[i] = LD16(rs, bo + i * 1536);
                }
                float s4[4];
#pragma unroll
                for (int e = 0; e < 4; ++e)
                    s4[e] = sadd(sadd(__uint_as_float(xr[0][e]), __uint_as_float(xr[1][e])), sadd(__uint_as_float(xr[2][e]), __uint_as_float(xr[3][e])));
                float s0 = fold2(s4[0], s4[2]); PIN(s0);
                float s1 = fold2(s4[1], s4[3]); PIN(s1);
                s0 = dpp_add<0x118>(s0); s1 = dpp_add<0x118>(s1);
                s0 = sadd(s0, __shfl_xor(s0, 16)); s1 = sadd(s1, __shfl_xor(s1, 16));
                if ((lane & 24) == 8) ST8(((u32x2){pk2(s0, s1), tag}), rs, WS_MOD + (192 * G + 16 * f + 2 * (lane & 7) + (lane >> 5)) * 8);
            }
        } else {
            pf = GLD(unsigned, pfa, pfo);
            CFENCE();
        }
        {
            STAMP(6);
            BAR();
            STAMP(7);
            f32x4 p[2][4];
#pragma unroll
            for (int i = 0; i < 2; ++i) {
                p[i][0] = LDS(f32x4, pr + i * 4608); p[i][1] = LDS(f32x4, pr + i * 4608 + 9216);
                p[i][2] = LDS(f32x4, pr + i * 4608 + 288); p[i][3] = LDS(f32x4, pr + i * 4608 + 9216 + 288);
            }
#pragma unroll
            for (int i = 0; i < 2; ++i) {
                u32x4 r;
#pragma unroll
                for (int e = 0; e < 4; ++e) {
                    const f32x4 pa = p[i][e & 2], pb = p[i][(e & 2) + 1];
                    const f32x2 v = ((f32x2){pa[2 * (e & 1)], pa[2 * (e & 1) + 1]} + (f32x2){kd[i][2 * e], kd[i][2 * e + 1]}) + (f32x2){pb[2 * (e & 1)], pb[2 * (e & 1) + 1]};
                    r[e] = pk2(v[0], v[1]);
                }
                ST16(r, rs, yo + i * 4096);
            }
        }
        CFENCE();
        STAMP(8);
        KEEP(pf);
        asm volatile("s_waitcnt vmcnt(2)" ::: "memory");
    } else {
        const int cw = wave - 4, tc = t - 256;
        const int chl = cw >> 1, h = cw & 1;
        u32x4 A[4][8];
        {
            const auto rk = __builtin_amdgcn_make_buffer_rsrc((void*)(ssmk + (ch0 + chl) * S), 0, S * 2, 0x00020000);
            const unsigned ko = g * 512 + h * 256 + a * 16;
            const u32x4 k0 = __builtin_amdgcn_raw_buffer_load_b128(rk, ko, 0, 0);
            const u32x4 k1 = __builtin_amdgcn_raw_buffer_load_b128(rk, ko + 2048, 0, 0);
            const u32x4 k2 = __builtin_amdgcn_raw_buffer_load_b128(rk, (lane >> 2) * 512 + h * 256 + (lane & 3) * 16 - 32, 0, 0);
            const unsigned wo = (16 * j + (lane >> 2)) * 6144 + 768 * G + 192 * cw + (lane & 3) * 16;
            const u32x2 cv = GLD(u32x2, cin, 32 * j + 8 * g);
            const u32x4 m0 = GLD(u32x4, wmod, wo), m1 = GLD(u32x4, wmod, wo + 64), m2 = GLD(u32x4, wmod, wo + 128);
            CFENCE();
            asm volatile("s_load_dword %0, %1, 0x0" : "=s"(ep) : "s"(ws) : "memory");
            if (cw == 0) LDS(u32x4, L_U + (lane >> 5) * U_STRIDE + (lane & 31) * 16) = (u32x4){0, 0, 0, 0};
            if (cw == 1) { LDS(unsigned, M_CAND + lane * 4) = 0u; LDS(u32x4, M_CNT0) = (u32x4){0, 0, 0, 0}; }
            BAR();
            asm volatile("" : "+s"(ep));
            tag = ep + 1u;
            const unsigned mpo = a < 6 ? WS_MP + (G * 32 + j) * 1536 + cw * 384 + a * 64 + g * 16 : WS_END;
            const unsigned uo = WS_U + (16 * G + (lane >> 2)) * 16384 + j * 512 + cw * 128 + (lane & 3) * 16;
            ST16(SENT4, rs, uo);
            ST16(SENT4, rs, uo + 64);
            ST16(SENT4, rs, mpo);
            if (j == 0 && tc == 64) ST4(0u, rs, WS_C2 + G * 128);
            const unsigned so = is_row ? (tc < 4 ? WS_R + (G * 16 + j) * 128 + tc * 16 : WS_END) : ((f < 7 && tc < 32) ? WS_ACT + (G * 7 + f) * 512 + tc * 16 : WS_END);
            ST16(SENT4, rs, so);
            CFENCE();
            const unsigned kc = L_KC + cw * KCW;
            const unsigned w0 = kc + g * 288 + 32 + a * 16, w2 = kc + (lane >> 2) * 288 + (lane & 3) * 16;
#define SHF(k, n) ((u32x4){__builtin_amdgcn_alignbit(k[1], k[0], 16), __builtin_amdgcn_alignbit(k[2], k[1], 16), __builtin_amdgcn_alignbit(k[3], k[2], 16), __builtin_amdgcn_alignbit(n, k[3], 16)})
#define NXT(k) ((unsigned)__builtin_amdgcn_update_dpp(0, (int)k[0], 0x101, 0xf, 0xf, true))
            LDS(u32x4, w0) = k0; LDS(u32x4, w0 + KC1) = SHF(k0, NXT(k0));
            LDS(u32x4, w0 + 1152) = k1; LDS(u32x4, w0 + 1152 + KC1) = SHF(k1, NXT(k1));
            const u32x4 k2s = SHF(k2, NXT(k2));
            if (lane < 32 && (lane & 2) == 0) { LDS(u32x4, w2) = k2; LDS(u32x4, w2 + KC1) = k2s; }
            const u32x2 sc = {pk2(silu(blo(cv[0])), silu(bhi(cv[0]))), pk2(silu(blo(cv[1])), silu(bhi(cv[1])))};
            const unsigned ml = L_T + cw * 3072;
            LDS(u32x4, ml + (lane >> 2) * 192 + (lane & 3) * 16) = m0;
            LDS(u32x4, ml + (lane >> 2) * 192 + (lane & 3) * 16 + 64) = m1;
            LDS(u32x4, ml + (lane >> 2) * 192 + (lane & 3) * 16 + 128) = m2;
            STAMP(1);
            DRAIN();
            if (lds_arrive<M_CNT0>() == 3u) ST4(tag, rs, lane == 0 ? WS_F1 + (G * 32 + j) * 128 : WS_END);
            STAMP(2);
            f32x4 ms = {0, 0, 0, 0};
#pragma unroll
            for (int i = 0; i < 6; ++i) {
                const f32x4 d = mfma16(trd(ml + (lane >> 2) * 192 + (lane & 3) * 8 + i * 32), sc, (f32x4){0, 0, 0, 0});
                if (a == i) ms = d;
            }
            ST16(__builtin_bit_cast(u32x4, ms), rs, mpo);
            CFENCE();
            STAMP(3);
            const int x0 = 25 + a - 8 * g;
            const int cpy = x0 & 1;
            const unsigned base = kc + cpy * KC1 + (x0 - cpy) * 2;
            const unsigned fo = lane < 8 ? WS_F1 + (G * 32 + 8 * cw + lane) * 128 : WS_END;
#pragma unroll
            for (int mu = 0; mu < 8; ++mu) {
#pragma unroll
                for (int r = 0; r < 4; ++r) {
                    const LAS unsigned* p = (const LAS unsigned*)(base + mu * 288 + r * 64);
                    A[r][mu] = (u32x4){p[0], p[1], p[2], p[3]};
                }
                if (mu == APOLL) {
#pragma unroll
                    for (int m2_ = 0; m2_ <= APOLL; ++m2_)
#pragma unroll
                        for (int r = 0; r < 4; ++r) PIN(A[r][m2_]);
                    SPIN {
                        CFENCE();
                        const unsigned v = LD4(rs, fo);
                        if ((__builtin_amdgcn_ballot_w64(v != tag) & 0xffull) == 0ull) break;
                    }
                    lds_arrive<M_CNT0 + 4>();
                }
            }
#pragma unroll
            for (int r = 0; r < 4; ++r)
#pragma unroll
                for (int mu = APOLL + 1; mu < 8; ++mu) PIN(A[r][mu]);
        }
        unsigned base = L_U + chl * U_STRIDE + U_PADB + (16 * (a - 1) + 8 * g - h * 8 * 16 - 96) * 2;
        unsigned pw = L_P + cw * 9216 + chl * 128 + g * 288 + a * 16;
        PIN(base); PIN(pw);
        f32x4 acc[8];
#pragma unroll
        for (int i = 0; i < 8; ++i) acc[i] = (f32x4){0, 0, 0, 0};
        STAMP(4);
        __builtin_amdgcn_s_setprio(3);
        BAR();
        STAMP(5);
        u32x4 B[2][8];
#pragma unroll
        for (int tau = 0; tau < 8; ++tau) B[0][tau] = LDS(u32x4, base + (256 * tau + 96) * 2);
        SCHED();
#pragma unroll
        for (int r = 0; r < 3; ++r) {
#pragma unroll
            for (int tau = 0; tau < 8; ++tau) {
#pragma unroll
                for (int mu = 0; mu + tau < 8; ++mu) {
                    acc[mu + tau] = mfma(A[r][mu], B[r & 1][tau], acc[mu + tau]);
                    if (tau == 1 || (tau == 2 && mu == 0)) {
                        const int n = tau == 1 ? mu : 7;
                        B[(r + 1) & 1][n] = LDS(u32x4, base + (256 * n + 96 - 32 * (r + 1)) * 2);
                        SCHED();
                    }
                }
                SCHED();
            }
        }
#pragma unroll
        for (int tt = 0; tt < 8; ++tt) {
#pragma unroll
            for (int mu = 0; mu <= tt; ++mu) {
                acc[tt] = mfma(A[3][mu], B[1][tt - mu], acc[tt]);
                if (mu == tt && tt >= 2) LDS(f32x4, pw + (tt - 2) * 1152) = acc[tt - 2];
                if (tt == 7 && mu == 3) LDS(f32x4, pw + 6 * 1152) = acc[6];
                SCHED();
            }
        }
        LDS(f32x4, pw + 7 * 1152) = acc[7];
        __builtin_amdgcn_s_setprio(0);
        STAMP(6);
        BAR();
        STAMP(7);
        if (is_row) {
            const unsigned ww = GLD(unsigned, wr, tc * 4);
            u32x2 m[6];
            SPIN {
                CFENCE();
                unsigned bad = 0;
#pragma unroll
                for (int i = 0; i < 6; ++i) m[i] = LD8(rs, WS_MOD + i * 2048 + tc * 8);
#pragma unroll
                for (int i = 0; i < 6; ++i) bad |= m[i][1] ^ tag;
                if (none(bad != 0u)) break;
                __builtin_amdgcn_s_sleep(8);
            }
            const f32x2 one = {1.f, 1.f};
            const f32x2 ca = b2(m[1][0]) + one, cb = b2(m[4][0]) + one, ga = b2(m[2][0]), sb = b2(m[3][0]), w = b2(ww);
            const f32x2 ca1 = b2(pk2(ca[0], ca[1])), cb1 = b2(pk2(cb[0], cb[1]));
            const f32x2 wc = w * cb1, sw = sb * w;
            LDS(f32x2, L_CH + tc * 8) = ga * ca1;
            LDS(f32x2, L_CH + 2048 + tc * 8) = ga * b2(m[0][0]);
            LDS(f32x2, L_CH + 4096 + tc * 8) = wc;
            LDS(f32x2, L_CH + 6144 + tc * 8) = cb1;
            LDS(f32x2, L_CH + 8192 + tc * 8) = sb;
            LDS(f32x2, L_CH + 10240 + tc * 8) = b2(m[5][0]);
            const float s = half_sum(fold2(wc[0] + wc[1], sw[0] + sw[1]));
            if ((lane & 31) == 31) LDS(float, M_MODP + (lane >> 5) * 16 + cw * 4) = s;
        }
        STAMP(8);
    }
    if (is_row) {
        const int rbk = G * 16 + j;
        const unsigned f2o = lane == 0 ? WS_F2 + (G * 8 + wave) * 128 : WS_END;
        const u32x4 x0 = LDS(u32x4, L_X + wave * 2048 + lane * 16), x1 = LDS(u32x4, L_X + wave * 2048 + 1024 + lane * 16);
        f32x2 nm, rs1;
        {
            float s0 = 0, s1 = 0, q0 = 0, q1 = 0;
#pragma unroll
            for (int e = 0; e < 4; ++e) {
                s0 = dot2(x0[e], 0x3f803f80u, s0); s1 = dot2(x1[e], 0x3f803f80u, s1);
                q0 = dot2(x0[e], x0[e], q0); q1 = dot2(x1[e], x1[e], q1);
            }
            const float mv = half_sum(fold2(s0, s1)) * (1.f / 512.f);
            const float q = half_sum(fold2(q0, q1)) * (1.f / 512.f);
            const float var = rb(fmaxf(q - mv * mv, 0.f));
            const float rstd = __builtin_amdgcn_rcpf(rb(__builtin_amdgcn_sqrtf(rb(var + 1e-5f))));
            const float nmv = -rb(mv) * rstd;
            nm = (f32x2){rdl(nmv, 31), rdl(nmv, 63)}; rs1 = (f32x2){rdl(rstd, 31), rdl(rstd, 63)};
            PINS(nm); PINS(rs1);
        }
        BAR();
        u32x2 fl = LD8(rs, f2o);
        CFENCE();
        STAMP(9);
        f32x2 x[8], gh[8];
        f32x4 wc[2];
        float Wc, Sb;
        {
            const f32x4 w4 = LDS(f32x4, M_MODP), s4 = LDS(f32x4, M_MODP + 16);
#pragma unroll
            for (int hh = 0; hh < 2; ++hh) {
                const f32x4 A1 = LDS(f32x4, L_CH + lane * 32 + hh * 16), A0 = LDS(f32x4, L_CH + 2048 + lane * 32 + hh * 16);
                wc[hh] = LDS(f32x4, L_CH + 4096 + lane * 32 + hh * 16);
#pragma unroll
                for (int e4 = 0; e4 < 4; ++e4) {
                    const int e = hh * 4 + e4;
                    x[e] = (e & 1) ? (f32x2){bhi(x0[e >> 1]), bhi(x1[e >> 1])} : (f32x2){blo(x0[e >> 1]), blo(x1[e >> 1])};
                    gh[e] = pfma(pfma(x[e], rs1, nm), (f32x2){A1[e4], A1[e4]}, (f32x2){A0[e4], A0[e4]});
                    PIN(x[e]); PIN(gh[e]);
                }
                PIN(wc[hh]);
            }
            Wc = (w4[0] + w4[1]) + (w4[2] + w4[3]); Sb = (s4[0] + s4[1]) + (s4[2] + s4[3]);
            PIN(Wc); PIN(Sb);
        }
        unsigned yo = WS_Y + (wave * 32 + (lane >> 2)) * 8192 + rbk * 64 + (lane & 3) * 16;
        unsigned yo2 = yo + 16 * 8192;
        unsigned tpo = L_T + ((lane & 1) * 4 * 516 + wave * 64 + (lane >> 1)) * 4;
        unsigned tro = L_T + wave * 2064 + lane * 32;
        unsigned kb = 0x800u + 2047u - (rbk * 16 + a);
        unsigned ko = M_SUMS + wave * 8 + (lane >> 5) * 4;
        unsigned kr = M_SUMS + a * 4;
        unsigned kg = lane < 16 ? WS_R + rbk * 128 + lane * 4 : WS_END;
        PIN(yo); PIN(yo2); PIN(tpo); PIN(tro); PIN(kb); PIN(ko); PIN(kr); PIN(kg);
        {
            SPIN {
                if (__builtin_amdgcn_readfirstlane(fl[0]) == tag) break;
                CFENCE();
                fl = LD8(rs, f2o);
            }
            wait_until(__builtin_amdgcn_readfirstlane(fl[1]) + D2);
            STAMP(10);
            u32x4 y0, y1;
            SPIN {
                CFENCE();
                y0 = LD16(rs, yo); y1 = LD16(rs, yo2);
                if (none(max4(y0, max4(y1, 0u)) == SENT)) break;
                __builtin_amdgcn_s_sleep(2);
            }
#pragma unroll
            for (int d = 0; d < 4; ++d) { LDS(unsigned, tpo + d * 2064) = y0[d]; LDS(unsigned, tpo + d * 2064 + 128) = y1[d]; }
        }
        STAMP(11);
        BAR();
        float mv, rstd;
        {
            const u32x4 ya = LDS(u32x4, tro), yc = LDS(u32x4, tro + 16);
            f32x2 s1, s2, s3;
#pragma unroll
            for (int e = 0; e < 8; ++e) {
                x[e] = pfma(gh[e], b2(e < 4 ? ya[e & 3] : yc[e & 3]), x[e]);
                const float w = wc[e >> 2][e & 3];
                if (e == 0) { s1 = x[0]; s2 = x[0] * x[0]; s3 = x[0] * w; }
                else { s1 += x[e]; s2 = pfma(x[e], x[e], s2); s3 = pfma(x[e], (f32x2){w, w}, s3); }
            }
            const float v1 = half_sum(fold2(s1[0], s1[1])), v2 = half_sum(fold2(s2[0], s2[1])), v3 = half_sum(fold2(s3[0], s3[1]));
            if ((lane & 31) == 31) { LDS(float, ko) = v1; LDS(float, ko + 64) = v2; LDS(float, ko + 128) = v3; }
        }
        BAR();
        {
            mv = LDS(float, kr) * (1.f / 512.f);
            const float q = LDS(float, kr + 64) * (1.f / 512.f);
            const float s = LDS(float, kr + 128);
            const float var = rb(fmaxf(q - mv * mv, 0.f));
            rstd = __builtin_amdgcn_rcpf(rb(__builtin_amdgcn_sqrtf(rb(var + 1e-5f))));
            if (wave == 0) {
                const float r = rstd * (s - mv * Wc) + Sb;
                const int u = (int)(pk2(r, r) & 0xffff0000u);
                const unsigned ord = ((unsigned)u ^ ((unsigned)(u >> 31) | 0x80000000u)) >> 16;
                ST4((ord << 12) + kb, rs, kg);
            }
        }
        CFENCE();
        STAMP(12);
        const auto ro = __builtin_amdgcn_make_buffer_rsrc(out, 0, S * D * 2, 0x00020000);
        {
            const float nmv = -mv * rstd;
            const f32x2 r2 = {rdl(rstd, 2 * wave), rdl(rstd, 2 * wave + 1)}, n2 = {rdl(nmv, 2 * wave), rdl(nmv, 2 * wave + 1)};
            f32x2 h2[8];
            f32x4 gb[2];
#pragma unroll
            for (int hh = 0; hh < 2; ++hh) {
                const f32x4 cb1 = LDS(f32x4, L_CH + 6144 + lane * 32 + hh * 16), sb = LDS(f32x4, L_CH + 8192 + lane * 32 + hh * 16);
                gb[hh] = LDS(f32x4, L_CH + 10240 + lane * 32 + hh * 16);
#pragma unroll
                for (int e4 = 0; e4 < 4; ++e4)
                    h2[hh * 4 + e4] = pfma(pfma(x[hh * 4 + e4], r2, n2), (f32x2){cb1[e4], cb1[e4]}, (f32x2){sb[e4], sb[e4]});
            }
            const unsigned oo = (rbk * 16 + 2 * wave) * 1024 + lane * 16;
#pragma unroll
            for (int i = 0; i < 2; ++i)
                ST16(((u32x4){pk2(h2[0][i], h2[1][i]), pk2(h2[2][i], h2[3][i]), pk2(h2[4][i], h2[5][i]), pk2(h2[6][i], h2[7][i])}), rs, WS_H + oo + i * 1024);
#pragma unroll
            for (int e = 0; e < 8; ++e) { const float gv = gb[e >> 2][e & 3]; x[e] = pfma(h2[e], (f32x2){gv, gv}, x[e]); }
#pragma unroll
            for (int i = 0; i < 2; ++i)
                ST16(((u32x4){pk2(x[0][i], x[1][i]), pk2(x[2][i], x[3][i]), pk2(x[4][i], x[5][i]), pk2(x[6][i], x[7][i])}), ro, oo + i * 1024);
        }
        DRAIN();
        BAR();
        if (t == 0) ST4(tag, rs, WS_F3 + rbk * 128);
        STAMP(13);
    } else {
        if (f >= 11) return;
        const bool is_gu = f < 7;
        u32x4 Wf[8];
        {
            const unsigned wl = L_W + wave * 8192;
            if (is_gu) {
                const u16* wp = wave < 4 ? wg : wu;
                const unsigned wo = ((wave & 3) * 128 + (lane >> 2)) * (F * 2) + (224 * G + 32 * f + 8 * (lane & 3)) * 2;
                u32x4 W[8];
#pragma unroll
                for (int i = 0; i < 8; ++i) W[i] = GLD(u32x4, wp, wo + i * 16 * (F * 2));
#pragma unroll
                for (int i = 0; i < 8; ++i) LDS(u32x4, wl + i * 1024 + lane * 16) = W[i];
                const unsigned rp = wl + (8 * g + (a >> 2)) * 64 + (lane & 3) * 8;
#pragma unroll
                for (int c = 0; c < 2; ++c)
#pragma unroll
                    for (int ks = 0; ks < 4; ++ks) Wf[c * 4 + ks] = trd2(rp + ks * 2048 + c * 32, 256);
            } else {
                const unsigned wo = (wave * 224 + (lane >> 1)) * (D * 2) + (64 * G + 16 * (f - 7) + 8 * (lane & 1)) * 2;
                u32x4 W[7];
#pragma unroll
                for (int i = 0; i < 7; ++i) W[i] = GLD(u32x4, wd, wo + i * 32 * (D * 2));
#pragma unroll
                for (int i = 0; i < 7; ++i) LDS(u32x4, wl + i * 1024 + lane * 16) = W[i];
                const unsigned rp = wl + (8 * g + (a >> 2)) * 32 + (lane & 3) * 8;
#pragma unroll
                for (int ks = 0; ks < 7; ++ks) Wf[ks] = trd2(rp + ks * 1024, 128);
                Wf[7] = Wf[0];
            }
#pragma unroll
            for (int i = 0; i < 8; ++i) PIN(Wf[i]);
        }
        STAMP(9);
        unsigned ro_ = WS_R + (t >> 2) * 128 + (t & 3) * 16, cko = M_CAND + (t >> 3) * 4, cco = M_CAND + (t & 7) * 32, xw = L_X + wave * 1040 + lane * 16;
        unsigned hb = WS_H + 2047 * 1024 + 0x800 * 1024 + lane * 16, xrd = L_X + (a & 7) * 1040 + (wave & 3) * 256 + g * 16;
        unsigned pwo = is_gu ? L_P + (wave * 8 + a) * 144 + g * 16 : L_P + (wave * 8 + a) * 80 + g * 16;
        PIN(ro_); PIN(cko); PIN(cco); PIN(xw); PIN(xrd); PIN(hb); PIN(pwo);
        {
            u32x2 fl;
            SPIN {
                CFENCE();
                fl = LD8(rs, lane < 8 ? WS_F2 + (G * 8 + lane) * 128 : WS_END);
                if ((__builtin_amdgcn_ballot_w64(fl[0] != tag) & 0xffull) == 0ull) break;
                __builtin_amdgcn_s_sleep(4);
            }
            const unsigned now = rt32();
            const unsigned tl = wave_max(lane < 8 ? fl[1] - now + 0x40000000u : 0u) - 0x40000000u + now;
            wait_until(tl + D3);
        }
        STAMP(10);
        {
            u32x4 key;
            unsigned lm;
            SPIN {
                CFENCE();
                key = LD16(rs, ro_);
                lm = max4(key, 0u);
                if (none(lm == SENT)) break;
            }
            STAMP(11);
            const unsigned wm_ = wave_max_v(lm);
            if (lane == 63) LDS(unsigned, M_WMAX + wave * 4) = wm_;
            const unsigned a0 = max(key[0], key[1]), a1 = min(key[0], key[1]), a2 = max(key[2], key[3]), a3 = min(key[2], key[3]);
            const unsigned m1 = min(a0, a2), m2 = max(a1, a3);
            unsigned k0 = lm, k1 = max(m1, m2), k2 = min(m1, m2), k3 = min(a1, a3);
            BAR();
            const u32x4 ma = LDS(u32x4, M_WMAX), mb = LDS(u32x4, M_WMAX + 16);
            unsigned T = min(min(min(ma[0], ma[1]), min(ma[2], ma[3])), min(min(mb[0], mb[1]), min(mb[2], mb[3])));
            const u64 b0 = __builtin_amdgcn_ballot_w64(k0 >= T), b1 = __builtin_amdgcn_ballot_w64(k1 >= T);
            const int n0 = __builtin_popcountll(b0);
            if (__builtin_amdgcn_ballot_w64(k2 >= T) == 0ull && n0 + __builtin_popcountll(b1) <= 8) {
                const int pos = __builtin_amdgcn_mbcnt_hi((unsigned)(b0 >> 32), __builtin_amdgcn_mbcnt_lo((unsigned)b0, wave * 8));
                if (k0 >= T) LDS(unsigned, M_CAND + pos * 4) = k0;
                if (b1 != 0ull) {
                    const int pos1 = __builtin_amdgcn_mbcnt_hi((unsigned)(b1 >> 32), __builtin_amdgcn_mbcnt_lo((unsigned)b1, wave * 8 + n0));
                    if (k1 >= T) LDS(unsigned, M_CAND + pos1 * 4) = k1;
                }
            } else {
#pragma unroll 1
                for (int i = 0; i < 8; ++i) {
                    const unsigned mx = wave_max(k0);
                    if (mx < __builtin_amdgcn_readfirstlane(T)) break;
                    if (k0 == mx) { LDS(unsigned, M_CAND + (wave * 8 + i) * 4) = mx; k0 = k1; k1 = k2; k2 = k3; k3 = 0u; }
                }
            }
            BAR();
            {
                const unsigned kx = LDS(unsigned, cko);
                const u32x4 c0 = LDS(u32x4, cco), c1 = LDS(u32x4, cco + 16);
                unsigned bits = 0;
#pragma unroll
                for (int e = 0; e < 4; ++e) bits = __builtin_amdgcn_alignbit(bits, kx - c0[e], 31);
#pragma unroll
                for (int e = 0; e < 4; ++e) bits = __builtin_amdgcn_alignbit(bits, kx - c1[e], 31);
                int rank = __builtin_popcount(bits);
                rank += __builtin_amdgcn_update_dpp(0, rank, 0xb1, 0xf, 0xf, true);
                rank += __builtin_amdgcn_update_dpp(0, rank, 0x4e, 0xf, 0xf, true);
                rank += __builtin_amdgcn_update_dpp(0, rank, 0x141, 0xf, 0xf, true);
                if (rank < 8) LDS(unsigned, M_SEL + rank * 4) = kx;
            }
            BAR();
        }
        STAMP(12);
        if (is_gu) {
            const unsigned ho = hb - ((LDS(unsigned, M_SEL + wave * 4) & 0xfffu) << 10);
            u32x4 hv;
            SPIN {
                CFENCE();
                hv = LD16(rs, ho);
                if (none(max4(hv, 0u) == SENT)) break;
            }
            LDS(u32x4, xw) = hv;
            STAMP(13);
            BAR();
            u32x4 xa[4];
#pragma unroll
            for (int ks = 0; ks < 4; ++ks) xa[ks] = LDS(u32x4, xrd + ks * 64);
            f32x4 acc0 = {0, 0, 0, 0}, acc1 = {0, 0, 0, 0};
#pragma unroll
            for (int ks = 0; ks < 4; ++ks) { acc0 = mfma(Wf[ks], xa[ks], acc0); acc1 = mfma(Wf[4 + ks], xa[ks], acc1); }
            if (a < 8) { LDS(f32x4, pwo) = acc0; LDS(f32x4, pwo + 64) = acc1; }
            BAR();
            if (wave < 4) {
                const unsigned pp = L_P + (lane >> 3) * 144 + (wave * 8 + (lane & 7)) * 4;
                float v[8];
#pragma unroll
                for (int w = 0; w < 8; ++w) v[w] = LDS(float, pp + w * 1152);
                const float gp = rb((v[0] + v[1]) + (v[2] + v[3])), up = rb((v[4] + v[5]) + (v[6] + v[7]));
                const float o = rb(silu(gp)) * up;
                const float on = __int_as_float(__builtin_amdgcn_update_dpp(0, __float_as_int(o), 0xb1, 0xf, 0xf, true));
                ST4(pk2(o, on), rs, (t & 1) ? WS_END : WS_ACT + (G * 7 + f) * 512 + t * 2);
            }
            STAMP(14);
        } else {
            const unsigned tk = rt32();
            const auto ro = __builtin_amdgcn_make_buffer_rsrc(out, 0, S * D * 2, 0x00020000);
            const int col0 = 64 * G + 16 * (f - 7);
            float o0 = 0, o1 = 0, g0 = 0, g1 = 0;
            unsigned ooff = 0;
            if (wave == 0) {
                const unsigned fk = LDS(unsigned, M_SEL + (lane >> 3) * 4);
                const int frow = 2047 - (int)(fk & 0x7ffu);
                unsigned u = (fk >> 12) ^ 0x8000u;
                u = (u & 0x8000u) ? (u ^ 0x7fffu) : u;
                const float sig = rb(__builtin_amdgcn_rcpf(1.f + __expf(-blo(u))));
                u32x2 gbv;
                SPIN {
                    CFENCE();
                    gbv = LD8(rs, WS_MOD + (1280 + col0 / 2 + (lane & 7)) * 8);
                    const unsigned v = LD4(rs, WS_F3 + (frow >> 4) * 128);
                    if (none(((v ^ tag) | (gbv[1] ^ tag)) != 0u)) break;
                    __builtin_amdgcn_s_sleep(2);
                }
                CFENCE();
                ooff = (frow * D + col0 + 2 * (lane & 7)) * 2;
                const unsigned oprev = LD4(ro, ooff);
                o0 = blo(oprev); o1 = bhi(oprev); g0 = blo(gbv[0]) * sig; g1 = bhi(gbv[0]) * sig;
                PIN(o0); PIN(o1); PIN(g0); PIN(g1);
            }
            unsigned ao = a < 8 ? WS_ACT + wave * 7 * 512 + g * 128 + a * 16 : WS_END;
            unsigned pp = L_P + (lane >> 3) * 80 + (lane & 7) * 8;
            PIN(ao); PIN(pp);
            wait_until(tk + D4);
            u32x4 av[7];
            SPIN {
                CFENCE();
#pragma unroll
                for (int ks = 0; ks < 7; ++ks) av[ks] = LD16(rs, ao + ks * 512);
                unsigned m = 0;
#pragma unroll
                for (int ks = 0; ks < 7; ++ks) m = max4(av[ks], m);
                if (none(m == SENT)) break;
                __builtin_amdgcn_s_sleep(1);
            }
            STAMP(13);
            f32x4 acc0 = {0, 0, 0, 0};
#pragma unroll
            for (int ks = 0; ks < 7; ++ks) acc0 = mfma(Wf[ks], av[ks], acc0);
            if (a < 8) LDS(f32x4, pwo) = acc0;
            BAR();
            if (wave == 0) {
                f32x2 v[8];
#pragma unroll
                for (int w = 0; w < 8; ++w) v[w] = LDS(f32x2, pp + w * 640);
                const f32x2 s = ((v[0] + v[1]) + (v[2] + v[3])) + ((v[4] + v[5]) + (v[6] + v[7]));
                ST4(pk2(o0 + g0 * rb(s[0]), o1 + g1 * rb(s[1])), ro, ooff);
                if (G == 0 && f == 7 && lane == 0) ST4(tag, rs, WS_E);
            }
            STAMP(14);
        }
    }
}

extern "C" void kernel_launch(void* const* d_in, const int* in_sizes, int n_in,
                              void* d_out, int out_size, void* d_ws, size_t ws_size,
                              hipStream_t stream) {
    fused_kernel<<<256, 512, 0, stream>>>((const u16*)d_in[1], (const u16*)d_in[2], (char*)d_ws, (const u16*)d_in[4],
                                          (const u16*)d_in[3], (const u16*)d_in[11], (const u16*)d_in[12], (const u16*)d_in[5],
                                          (const u16*)d_in[0], (const u16*)d_in[7], (const u16*)d_in[8], (const u16*)d_in[9],
                                          (const u16*)d_in[10], (u16*)d_out);
}
```

```cpp
#include <hip/hip_runtime.h>
#include <stdint.h>
#pragma clang diagnostic ignored "-Wint-to-pointer-cast"

typedef unsigned short u16;
typedef unsigned long long u64;
typedef short bf16x8 __attribute__((ext_vector_type(8)));
typedef short s16x4 __attribute__((ext_vector_type(4)));
typedef __bf16 bf2 __attribute__((ext_vector_type(2)));
typedef float f32x4 __attribute__((ext_vector_type(4)));
typedef float f32x2 __attribute__((ext_vector_type(2)));
typedef unsigned u32x4 __attribute__((ext_vector_type(4)));
typedef unsigned u32x2 __attribute__((ext_vector_type(2)));
#define LAS __attribute__((address_space(3)))
#define GAS __attribute__((address_space(1)))

constexpr int S = 2048, D = 512, F = 1792;
constexpr unsigned SENT = 0xFFFFFFFFu;

constexpr unsigned WS_E = 0;
constexpr unsigned WS_F1 = 1024;
constexpr unsigned WS_F2 = WS_F1 + 256 * 128;
constexpr unsigned WS_F4 = WS_F2 + 64 * 128;
constexpr unsigned WS_F3 = WS_F4 + 256 * 32;
constexpr unsigned WS_MOD = WS_F3 + 128 * 128;
constexpr unsigned WS_R = WS_MOD + 12288;
constexpr unsigned WS_ACT = WS_R + 16384;
constexpr unsigned WS_MP = 131072;
constexpr unsigned WS_U = WS_MP + 393216;
constexpr unsigned WS_Y = WS_U + 2097152;
constexpr unsigned WS_H = WS_Y + 2097152;
constexpr unsigned WS_END = WS_H + 3145728;
constexpr unsigned WS_DBG = WS_END;
static_assert(WS_ACT + 28672 <= WS_MP, "ws layout");

constexpr int U_PADB = 512, U_STRIDE = 4672;
constexpr int L_U = 0;
constexpr int L_P = 9344;
constexpr int L_MISC = L_P + 4 * 9216 + 128;
constexpr int L_S = L_MISC + 2048;
constexpr int KROW = 192, SW = 36 * KROW;
constexpr int KC1 = 2368, KCW = 4736;
constexpr int L_KC = L_S + 32768;
constexpr int L_T = L_KC + 4 * KCW;
constexpr int L_CH = L_T + 8 * 2064;
constexpr int L_X = L_S;
constexpr int L_W = L_S;
constexpr int L_END = L_CH + 12288;
static_assert(L_W + 65536 <= L_CH, "lds layout");
static_assert(L_END <= 160 * 1024 && L_END > 80 * 1024, "lds size");
constexpr int M_WMAX = L_MISC;
constexpr int M_SEL = L_MISC + 64;
constexpr int M_TY = L_MISC + 112;
constexpr int M_CNT0 = L_MISC + 96;
constexpr int M_LN1 = L_MISC + 384;
constexpr int M_SUMS = L_MISC + 128;
constexpr int M_CAND = L_MISC + 768;
constexpr int M_DUMMY = L_MISC + 1024;
constexpr int M_MODP = L_MISC + 512;

#define CPF_LANE (t - 256)
#define CODE_LINES 144
#define PFW 1
#define DP 0
#define DW 128
#define DF 175
#define PT 1
#define DO 10
#define DK 0
#define DM 110
#define D2 235
#define D3 0
#define D4 0
constexpr unsigned SPIN_CAP = 40000;

__device__ __forceinline__ float blo(unsigned w) { return __uint_as_float(w << 16); }
__device__ __forceinline__ float bhi(unsigned w) { return __uint_as_float(w & 0xffff0000u); }
__device__ __forceinline__ f32x2 b2(unsigned w) { return (f32x2){blo(w), bhi(w)}; }
__device__ __forceinline__ float rb(float f) { return (float)(__bf16)f; }
__device__ __forceinline__ unsigned pk2(float lo, float hi) {
    bf2 v; v[0] = (__bf16)lo; v[1] = (__bf16)hi;
    return __builtin_bit_cast(unsigned, v);
}
__device__ __forceinline__ float dot2(unsigned a, unsigned b, float c) {
    return __builtin_amdgcn_fdot2_f32_bf16(__builtin_bit_cast(bf2, a), __builtin_bit_cast(bf2, b), c, false);
}
__device__ __forceinline__ f32x2 pfma(f32x2 a, f32x2 b, f32x2 c) { return __builtin_elementwise_fma(a, b, c); }
__device__ __forceinline__ unsigned rt32() { return (unsigned)__builtin_amdgcn_s_memrealtime(); }
#define BAR() do { asm volatile("s_waitcnt lgkmcnt(0)" ::: "memory"); __builtin_amdgcn_s_barrier(); asm volatile("" ::: "memory"); } while (0)
#define DRAIN() asm volatile("s_waitcnt vmcnt(0)" ::: "memory")
#define CFENCE() asm volatile("" ::: "memory")
#define PIN(x) asm volatile("" : "+v"(x))
#define PINS(x) asm volatile("" : "+s"(x))
#define KEEP(x) asm volatile("" :: "v"(x))
#define SCHED() __builtin_amdgcn_sched_barrier(0)
#define SPIN _Pragma("unroll 1") for (unsigned sp_ = 0; sp_ < SPIN_CAP; ++sp_)
#define LDS(T, off) (*(LAS T*)(unsigned)(off))
template <int OFF>
__device__ __forceinline__ unsigned lds_arrive() {
    unsigned old; u64 ex;
    asm volatile("s_mov_b64 %1, exec\n\ts_mov_b64 exec, 1\n\tds_add_rtn_u32 %0, %2, %3 offset:%4\n\ts_mov_b64 exec, %1\n\ts_waitcnt lgkmcnt(0)"
                 : "=&v"(old), "=&s"(ex) : "v"(0u), "v"(1u), "i"(OFF) : "memory");
    return __builtin_amdgcn_readfirstlane(old);
}
template <int OFF>
__device__ __forceinline__ void lds_signal() {
    u64 ex;
    asm volatile("s_mov_b64 %0, exec\n\ts_mov_b64 exec, 1\n\tds_add_u32 %1, %2 offset:%3\n\ts_mov_b64 exec, %0"
                 : "=&s"(ex) : "v"(0u), "v"(1u), "i"(OFF) : "memory");
}
template <int OFF>
__device__ __forceinline__ void lds_wait(unsigned want) {
#pragma unroll 1
    for (unsigned sp_ = 0; sp_ < 4000000u; ++sp_) { CFENCE(); if (__builtin_amdgcn_readfirstlane(LDS(volatile unsigned, OFF)) >= want) break; }
    CFENCE();
}
__device__ __forceinline__ unsigned mktag(unsigned e) { return e + 1u + (e == SENT - 1u); }
__device__ __forceinline__ unsigned rfl(unsigned v) { unsigned r; asm volatile("v_readfirstlane_b32 %0, %1" : "=s"(r) : "v"(v)); return r; }
__device__ __forceinline__ float smul(float a, float b) { float r; asm("v_mul_f32 %0, %1, %2" : "=v"(r) : "v"(a), "v"(b)); return r; }
__device__ __forceinline__ float sadd(float a, float b) { float r; asm("v_add_f32 %0, %1, %2" : "=v"(r) : "v"(a), "v"(b)); return r; }

template <int CTRL, int RMASK = 0xf>
__device__ __forceinline__ float dpp_add(float v) {
    return v + __int_as_float(__builtin_amdgcn_update_dpp(0, __float_as_int(v), CTRL, RMASK, 0xf, true));
}
__device__ __forceinline__ float half_sum(float v) {
    v = dpp_add<0x111>(v); v = dpp_add<0x112>(v); v = dpp_add<0x114>(v); v = dpp_add<0x118>(v);
    return dpp_add<0x142>(v);
}
__device__ __forceinline__ float fold2(float a, float b) {
    auto r = __builtin_amdgcn_permlane32_swap(__float_as_uint(a), __float_as_uint(b), false, false);
    return __uint_as_float(r[0]) + __uint_as_float(r[1]);
}
__device__ __forceinline__ float fold16(float a, float b) {
    auto r = __builtin_amdgcn_permlane16_swap(__float_as_uint(a), __float_as_uint(b), false, false);
    return __uint_as_float(r[0]) + __uint_as_float(r[1]);
}
__device__ __forceinline__ float row_sum(float v) {
    v = dpp_add<0x111>(v); v = dpp_add<0x112>(v); v = dpp_add<0x114>(v); return dpp_add<0x118>(v);
}
__device__ __forceinline__ float rdl(float v, int l) { return __int_as_float(__builtin_amdgcn_readlane(__float_as_int(v), l)); }
template <int CTRL, int RMASK = 0xf>
__device__ __forceinline__ unsigned dpp_max(unsigned v) {
    return max(v, (unsigned)__builtin_amdgcn_update_dpp(0, (int)v, CTRL, RMASK, 0xf, true));
}
__device__ __forceinline__ unsigned wave_max_v(unsigned v) {
    v = dpp_max<0x111>(v); v = dpp_max<0x112>(v); v = dpp_max<0x114>(v); v = dpp_max<0x118>(v);
    v = dpp_max<0x142>(v); v = dpp_max<0x143>(v);
    return v;
}
__device__ __forceinline__ unsigned row_max(unsigned v) {
    v = dpp_max<0x111>(v); v = dpp_max<0x112>(v); v = dpp_max<0x114>(v); v = dpp_max<0x118>(v);
    return (unsigned)__builtin_amdgcn_readlane((int)v, 15);
}
__device__ __forceinline__ unsigned wave_max(unsigned v) { return (unsigned)__builtin_amdgcn_readlane((int)wave_max_v(v), 63); }
__device__ __forceinline__ u32x2 trd(unsigned off) {
    return __builtin_bit_cast(u32x2, __builtin_amdgcn_ds_read_tr16_b64_v4i16((LAS s16x4*)off));
}
__device__ __forceinline__ u32x4 trd2(unsigned off, unsigned d) { const u32x2 lo = trd(off), hi = trd(off + d); return (u32x4){lo[0], lo[1], hi[0], hi[1]}; }
__device__ __forceinline__ f32x4 mfma(u32x4 a, u32x4 b, f32x4 c) {
    return __builtin_amdgcn_mfma_f32_16x16x32_bf16(__builtin_bit_cast(bf16x8, a), __builtin_bit_cast(bf16x8, b), c, 0, 0, 0);
}
__device__ __forceinline__ f32x4 mfma16(u32x2 a, u32x2 b, f32x4 c) {
    return __builtin_amdgcn_mfma_f32_16x16x16bf16_1k(__builtin_bit_cast(s16x4, a), __builtin_bit_cast(s16x4, b), c, 0, 0, 0);
}
__device__ __forceinline__ float silu(float x) { return x * __builtin_amdgcn_rcpf(1.f + __expf(-x)); }
__device__ __forceinline__ unsigned max3u(unsigned a, unsigned b, unsigned c) { return max(max(a, b), c); }
__device__ __forceinline__ unsigned max4(u32x4 v, unsigned m) { return max3u(max3u(v[0], v[1], m), v[2], v[3]); }
__device__ __forceinline__ bool none(bool c) { return __builtin_amdgcn_ballot_w64(c) == 0ull; }
__device__ __forceinline__ void wait_until(unsigned t) {
    const unsigned now = rt32();
    if ((int)(t - now) > 400) t = now + 400;
#pragma unroll 1
    while ((int)(rt32() - t) < 0) {}
}

#define STAMP(i) do {} while (0)
#define STAMP_OUT() do {} while (0)

#define LD16(rs, off) __builtin_amdgcn_raw_buffer_load_b128(rs, off, 0, 16)
#define LD8(rs, off) __builtin_amdgcn_raw_buffer_load_b64(rs, off, 0, 16)
#define LD4(rs, off) __builtin_amdgcn_raw_buffer_load_b32(rs, off, 0, 16)
#define ST16(v, rs, off) __builtin_amdgcn_raw_buffer_store_b128(v, rs, off, 0, 16)
#define ST8(v, rs, off) __builtin_amdgcn_raw_buffer_store_b64(v, rs, off, 0, 16)
#define ST4(v, rs, off) __builtin_amdgcn_raw_buffer_store_b32(v, rs, off, 0, 16)
#define ABL(op, d, off, imm) asm volatile("buffer_load_" op " %0, %1, %2, 0 offen offset:%3 sc1" : "=v"(d) : "v"(off), "s"(rq), "i"(imm) : "memory")
#define ABLS(op, d, off, soff) asm volatile("buffer_load_" op " %0, %1, %2, %3 offen sc1" : "=v"(d) : "v"(off), "s"(rq), "s"(soff) : "memory")
#define AGL(op, d, p, off, imm) asm volatile("global_load_" op " %0, %1, %2 offset:%3" : "=v"(d) : "v"(off), "s"(p), "i"(imm) : "memory")
#define VMW(n) asm volatile("s_waitcnt vmcnt(" #n ")" ::: "memory")
#define SENT4 ((u32x4){SENT, SENT, SENT, SENT})
#define GLD(T, p, off) (*(const GAS T*)((const GAS char*)(p) + (off)))

__global__ void __launch_bounds__(512, 2)
fused_kernel(const u16* __restrict__ kin, const u16* __restrict__ vin, char* __restrict__ ws, const u16* __restrict__ ssmk,
             const u16* __restrict__ shk, const u16* __restrict__ cin, const u16* __restrict__ wmod, const u16* __restrict__ ssmD,
             const u16* __restrict__ xq, const u16* __restrict__ wr, const u16* __restrict__ wg, const u16* __restrict__ wu,
             const u16* __restrict__ wd, u16* __restrict__ out) {
    __shared__ __attribute__((aligned(16))) char lds_[L_END];
    asm volatile("" :: "v"((LAS char*)lds_) : "memory");
    STAMP(0);
    int b = blockIdx.x;
    const int t = threadIdx.x, lane = t & 63;
    int wave = __builtin_amdgcn_readfirstlane(t >> 6);
    u32x4 ka, va, kb, vb, kh;
    u32x2 shw;
    unsigned cpf;
    {
        const unsigned toff = (b >> 3) * 65536 + (b & 7) * 128 + wave * 16384 + (lane >> 3) * 1024 + (lane & 7) * 16;
        u64 pc; asm volatile("s_getpc_b64 %0" : "=s"(pc));
        if (wave < 4) {
            AGL("dwordx4", ka, kin, toff, 0); AGL("dwordx4", va, vin, toff, 0);
            AGL("dwordx4", kb, kin, toff + 8192, 0); AGL("dwordx4", vb, vin, toff + 8192, 0);
            AGL("dwordx4", kh, kin, __builtin_elementwise_sub_sat(toff, 4096u), 0);
            AGL("dwordx2", shw, shk, (b & 7) * 512 + lane * 8, 0);
            CFENCE();
            __builtin_amdgcn_s_barrier();
        } else {
            AGL("dword", cpf, pc & ~(u64)255, min((b >> 3) + 32 * min(CPF_LANE, 4), CODE_LINES - 1) * 128, 0);
            LDS(u32x4, M_CNT0) = (u32x4){0, 0, 0, 0};
            BAR();
        }
        CFENCE();
    }
    PINS(b); PINS(wave);
    PINS(ws); PINS(ssmk); PINS(cin); PINS(wmod); PINS(ssmD);
    PINS(xq); PINS(wr); PINS(wg); PINS(wu); PINS(wd); PINS(out);
    unsigned ep;
    AGL("dword", ep, ws, 0u, WS_E);
    const int G = b & 7, j = b >> 3;
    const int ch0 = 64 * G + 2 * j;
    const auto rs = __builtin_amdgcn_make_buffer_rsrc(ws, 0, WS_END, 0x00020000);
    const u32x4 rq = {(unsigned)(u64)ws, (unsigned)((u64)ws >> 32) & 0xffffu, WS_END, 0x00020000u};
    STAMP(15);
    const bool is_row = j < 16;
    const int f = j - 16;
    const int rbk = G * 16 + j;
    const int a = lane & 15, g = lane >> 4;
    unsigned tag;
    u32x4 y[4];
    unsigned yl;
#define YLD() do { ABL("dwordx4", y[0], yl, 0); ABLS("dwordx4", y[1], yl, 131072u); ABLS("dwordx4", y[2], yl, 262144u); ABLS("dwordx4", y[3], yl, 393216u); } while (0)
    const int rp = 2 * (wave & 3) + (wave >> 2);
    unsigned tro, kb_, ko, kr, kg, cho;
    f32x2 x[8];
#define ROWPREP() do { \
        tro = L_T + rp * 2064 + lane * 32; kb_ = 0x800u + 2047u - (rbk * 16 + a); ko = M_SUMS + rp * 8 + (lane >> 5) * 4; kr = M_SUMS + a * 4; \
        kg = lane < 16 ? WS_R + rbk * 128 + lane * 4 : WS_END; cho = L_CH + lane * 32; \
        PIN(tro); PIN(kb_); PIN(ko); PIN(kr); PIN(kg); PIN(cho); \
        const unsigned xo_ = L_S + (wave & 3) * SW + (wave >> 2) * 2048 + lane * 16; \
        const u32x4 x0_ = LDS(u32x4, xo_), x1_ = LDS(u32x4, xo_ + 1024); \
        _Pragma("unroll") for (int e = 0; e < 8; ++e) x[e] = (e & 1) ? (f32x2){bhi(x0_[e >> 1]), bhi(x1_[e >> 1])} : (f32x2){blo(x0_[e >> 1]), blo(x1_[e >> 1])}; \
        _Pragma("unroll") for (int e = 0; e < 8; ++e) PIN(x[e]); } while (0)
    unsigned tpo[4];
    if (wave < 4) {
        const int hw = wave;
        unsigned yo = WS_Y + (G * 32 + j) * 8192 + t * 16;
        const int chl = (t >> 1) & 1, half = t & 1;
        unsigned dw, ww;
        AGL("ushort", dw, ssmD, (ch0 + chl) * 2, 0);
        AGL("dword", ww, wr, t * 4, 0);
        unsigned bo = is_row ? (rbk * 16 + 4 * hw) * 1024 + lane * 16
                             : WS_MP + (G * 32 + 4 * (lane >> 3)) * 1536 + f * 128 + (lane & 7) * 16;
        unsigned fo = lane < 8 ? WS_F1 + (G * 32 + 8 * hw + lane) * 128 : WS_END;
        unsigned ub = WS_U + (16 * G + (j >> 1)) * 16384 + (j & 1) * 32 + (hw * 64 + (lane >> 1)) * 64 + (lane & 1) * 16;
        unsigned ul = L_U + (lane & 1) * U_STRIDE + U_PADB + (hw * 64 + (lane >> 1)) * 16;
        {
            unsigned uo = WS_U + (16 * G + (lane >> 2)) * 16384 + j * 512 + hw * 128 + (lane & 3) * 16;
            const int q = a >> 2;
            unsigned sw = L_S + hw * SW + (lane >> 3) * KROW + (lane & 7) * 16;
            unsigned ra = L_S + hw * SW + q * KROW + 32 * g + 8 * (lane & 3);
            unsigned rb_ = ra + (17 - (q >> 1)) * KROW;
            unsigned f1o = lane == 0 ? WS_F1 + (G * 32 + j) * 128 : WS_END;
            PIN(fo); PIN(ub); PIN(ul);
            f32x2 o[8];
#pragma unroll
            for (int i = 0; i < 8; ++i) { o[i] = (f32x2){0.f, 0.f}; PIN(o[i]); }
            PIN(sw); PIN(ra); PIN(rb_); PIN(uo); PIN(yo); PIN(bo); PIN(f1o);
            VMW(7); PIN(ka); PIN(va);
            LDS(u32x4, sw + 4 * KROW) = ka; LDS(u32x4, sw + 20 * KROW) = va;
            VMW(5); PIN(kb); PIN(vb);
            LDS(u32x4, sw + 12 * KROW) = kb; LDS(u32x4, sw + 28 * KROW) = vb;
            VMW(3); PIN(kh); PIN(shw);
            if (j == 0 && hw == 0) kh = (u32x4){0, 0, 0, 0};
            if (lane < 32) LDS(u32x4, sw) = kh;
            CFENCE();
            STAMP(1);
            const unsigned w32 = __builtin_amdgcn_alignbit(shw[1], shw[1], 16), w10 = __builtin_amdgcn_alignbit(shw[0], shw[0], 16);
            unsigned Q[18], V[8];
#pragma unroll
            for (int i = 0; i < 4; ++i) {
                const u32x2 qo = trd(ra + (1 + 4 * i) * KROW), qe = trd(ra + (2 + 4 * i) * KROW), vv = trd(ra + (20 + 4 * i) * KROW);
                Q[4 * i] = qo[0]; Q[4 * i + 2] = qo[1]; Q[4 * i + 1] = qe[0]; Q[4 * i + 3] = qe[1]; V[2 * i] = vv[0]; V[2 * i + 1] = vv[1];
            }
            { const u32x2 ql = trd(rb_); Q[16] = ql[0]; Q[17] = ql[1]; }
#pragma unroll
            for (int hh = 0; hh < 2; ++hh) {
#pragma unroll
                for (int i = 8 * hh; i < 8 * hh + 8; ++i) o[i >> 1][i & 1] = dot2(Q[i + 2], w10, dot2(Q[i], w32, o[i >> 1][i & 1]));
#pragma unroll
                for (int i = 4 * hh; i < 4 * hh + 4; ++i) o[i] *= b2(V[i]);
                const u32x4 r = {pk2(o[4 * hh + 3][1], o[4 * hh + 3][0]), pk2(o[4 * hh + 2][1], o[4 * hh + 2][0]), pk2(o[4 * hh + 1][1], o[4 * hh + 1][0]), pk2(o[4 * hh][1], o[4 * hh][0])};
                ST16(r, rs, uo + 64 * hh);
                SCHED();
            }
            CFENCE();
            STAMP(2);
            VMW(4);
            tag = mktag(rfl(ep));
            DRAIN();
            if (lds_arrive<M_CNT0>() == 7u) ST4(tag, rs, f1o);
            STAMP(3);
        }
        const u16* pfa = hw < 2 ? wg : wu;
        unsigned pfo = (32 * f + 16 * (hw & 1) + (lane >> 2)) * (F * 2) + ((448 * G) / 128 + (lane & 3)) * 128;
        unsigned pr = L_P + chl * (2 * 9216 + 128) + hw * 1152 + half * 576 + (lane >> 2) * 16;
        unsigned kvo = L_U + chl * U_STRIDE + U_PADB + (t >> 2) * 32 + half * 16;
        unsigned mo = WS_MOD + t * 8;
        unsigned c2o = (t == 0 && j < 8) ? WS_F2 + (j * 8 + G) * 128 : WS_END;
        unsigned f2o = lane < 8 ? WS_F2 + (G * 8 + lane) * 128 : WS_END;
        PIN(pfo); PIN(pr); PIN(kvo); PIN(mo); PIN(c2o); PIN(f2o);
        const bool owner = !is_row && hw == 3 && f < 12;
        u32x4 xr[4];
#define MPLD() do { ABL("dwordx4", xr[0], bo, 0); ABL("dwordx4", xr[1], bo, 1536); ABL("dwordx4", xr[2], bo, 3072); ABLS("dwordx4", xr[3], bo, 4608u); } while (0)
#define XLD() asm volatile("s_mov_b32 m0, %2\n\ts_nop 0\n\tglobal_load_lds_dwordx4 %0, %1\n\tglobal_load_lds_dwordx4 %0, %1 offset:1024\n\t" \
                           "global_load_lds_dwordx4 %0, %1 offset:2048\n\tglobal_load_lds_dwordx4 %0, %1 offset:3072" :: "v"(bo), "s"(xq), "s"(L_S + hw * SW) : "memory")
        STAMP(4);
        if (DP) wait_until(rt32() + DP);
        SPIN {
            unsigned v;
            ABL("dword", v, fo, 0);
            VMW(0); PIN(v);
            if ((__builtin_amdgcn_ballot_w64(v != tag) & 0xffull) == 0ull) break;
        }
        STAMP(16);
        {
            u32x4 u0, u1;
            ABL("dwordx4", u0, ub, 0); ABL("dwordx4", u1, ub, 2048);
            lds_signal<M_CNT0 + 4>();
            lds_wait<M_CNT0 + 4>(4u);
            ST8(((u32x2){tag, rt32()}), rs, c2o);
            CFENCE();
            if (owner) MPLD();
            ST16(SENT4, rs, yo); ST16(SENT4, rs, yo + 4096);
            CFENCE();
            if (is_row) XLD();
            if (is_row || owner) VMW(7); else VMW(3);
            PIN(u0); PIN(u1);
            LDS(u32x4, ul) = u0; LDS(u32x4, ul + 512) = u1;
        }
        BAR();
        STAMP(5);
        const unsigned tconv = rt32();
        if (owner) {
            VMW(2);
            PIN(xr[0]); PIN(xr[1]); PIN(xr[2]); PIN(xr[3]);
            SPIN {
                if (none(max4(xr[0], max4(xr[1], max4(xr[2], max4(xr[3], 0u)))) == SENT)) break;
                MPLD();
                VMW(0); PIN(xr[0]); PIN(xr[1]); PIN(xr[2]); PIN(xr[3]);
            }
            float s4[4];
#pragma unroll
            for (int e = 0; e < 4; ++e)
                s4[e] = sadd(sadd(__uint_as_float(xr[0][e]), __uint_as_float(xr[1][e])), sadd(__uint_as_float(xr[2][e]), __uint_as_float(xr[3][e])));
            asm volatile("s_nop 1" : "+v"(s4[0]), "+v"(s4[1]), "+v"(s4[2]), "+v"(s4[3]));
            float s0 = fold2(s4[0], s4[2]); PIN(s0);
            float s1 = fold2(s4[1], s4[3]); PIN(s1);
            s0 = dpp_add<0x118>(s0); s1 = dpp_add<0x118>(s1);
            s0 = sadd(s0, __shfl_xor(s0, 16)); s1 = sadd(s1, __shfl_xor(s1, 16));
            ST8(((u32x2){pk2(s0, s1), tag}), rs, (lane & 24) == 8 ? WS_MOD + (192 * G + 16 * f + 2 * (lane & 7) + (lane >> 5)) * 8 : WS_END);
            VMW(1);
        } else if (is_row) VMW(4); else VMW(0);
        if (lds_arrive<M_CNT0 + 8>() == 3u) ST4(tag, rs, lane == 0 ? WS_F4 + (G * 32 + j) * 32 : WS_END);
        u32x2 fl;
        if (is_row) ABL("dwordx2", fl, f2o, 0);
        STAMP(18);
        PIN(dw); PIN(ww);
        const float Dv = blo(dw);
        float kd[2][8];
#pragma unroll
        for (int i = 0; i < 2; ++i) {
            const u32x4 kv = LDS(u32x4, kvo + i * 2048);
#pragma unroll
            for (int e = 0; e < 4; ++e) { kd[i][2 * e] = smul(bhi(kv[3 - e]), Dv); kd[i][2 * e + 1] = smul(blo(kv[3 - e]), Dv); PIN(kd[i][2 * e]); PIN(kd[i][2 * e + 1]); }
        }
        CFENCE();
        unsigned pf = 0;
        u32x2 m[6];
        unsigned tl = 0;
        bool early = false;
#define MODLD() do { ABL("dwordx2", m[0], mo, 0); ABL("dwordx2", m[1], mo, 2048); ABLS("dwordx2", m[2], mo, 4096u); ABLS("dwordx2", m[3], mo, 6144u); \
                     ABLS("dwordx2", m[4], mo, 8192u); ABLS("dwordx2", m[5], mo, 10240u); } while (0)
#define MODPIN() do { PIN(m[0]); PIN(m[1]); PIN(m[2]); PIN(m[3]); PIN(m[4]); PIN(m[5]); } while (0)
#define F2OK() ((__builtin_amdgcn_ballot_w64(fl[0] != tag) & 0xffull) == 0ull)
#define F2MAX() (row_max(lane < 8 ? fl[1] - tconv + 0x40000000u : 0u) + tconv - 0x40000000u)
#define TLOUT() do { LDS(unsigned, M_TY) = tl; LDS(unsigned, M_CNT0 + 12) = 1u; } while (0)
        if (is_row) {
            VMW(0); PIN(fl);
            if (!F2OK()) ABL("dwordx2", fl, f2o, 0);
            ROWPREP();
            STAMP(19);
            VMW(0); PIN(fl);
#pragma unroll 1
            while (!F2OK() && (int)(rt32() - tconv) < DW - 30) { ABL("dwordx2", fl, f2o, 0); VMW(0); PIN(fl); }
            if (F2OK()) {
                tl = F2MAX();
                TLOUT();
                if ((int)(tl + DM - tconv) < DW) { wait_until(tl + DM); MODLD(); early = true; }
            }
        } else if (PFW) { wait_until(tconv + PFW); AGL("dword", pf, pfa, pfo, 0); }
        {
            STAMP(6);
            BAR();
            STAMP(7);
            f32x4 p[2][4];
#pragma unroll
            for (int i = 0; i < 2; ++i) {
                p[i][0] = LDS(f32x4, pr + i * 4608); p[i][1] = LDS(f32x4, pr + i * 4608 + 9216);
                p[i][2] = LDS(f32x4, pr + i * 4608 + 288); p[i][3] = LDS(f32x4, pr + i * 4608 + 9216 + 288);
            }
#pragma unroll
            for (int i = 0; i < 2; ++i) {
                u32x4 r;
#pragma unroll
                for (int e = 0; e < 4; ++e) {
                    const f32x4 pa = p[i][e & 2], pb = p[i][(e & 2) + 1];
                    const f32x2 v = ((f32x2){pa[2 * (e & 1)], pa[2 * (e & 1) + 1]} + (f32x2){kd[i][2 * e], kd[i][2 * e + 1]}) + (f32x2){pb[2 * (e & 1)], pb[2 * (e & 1) + 1]};
                    r[e] = pk2(v[0], v[1]);
                }
                ST16(r, rs, yo + i * 4096);
            }
        }
        CFENCE();
        STAMP(8);
        VMW(2);
        KEEP(pf);
        if (is_row) {
            if (!early) {
                SPIN {
                    if (F2OK()) break;
                    ABL("dwordx2", fl, f2o, 0);
                    VMW(0); PIN(fl);
                }
                tl = F2MAX();
                TLOUT();
                wait_until(tl + DM);
                STAMP(22);
                MODLD();
                VMW(0);
            }
            MODPIN();
            SPIN {
                unsigned bad = 0;
#pragma unroll
                for (int i = 0; i < 6; ++i) bad |= m[i][1] ^ tag;
                if (__builtin_expect(none(bad != 0u), 1)) break;
                STAMP(21);
                __builtin_amdgcn_s_sleep(4);
                MODLD();
                VMW(0); MODPIN();
            }
            STAMP(17);
            const f32x2 one = {1.f, 1.f};
            const f32x2 ca = b2(m[1][0]) + one, cb = b2(m[4][0]) + one, ga = b2(m[2][0]), sb = b2(m[3][0]), w = b2(ww);
            const f32x2 ca1 = b2(pk2(ca[0], ca[1])), cb1 = b2(pk2(cb[0], cb[1]));
            const f32x2 wc = w * cb1, sw = sb * w;
            LDS(f32x2, L_CH + t * 8) = ga * ca1;
            LDS(f32x2, L_CH + 2048 + t * 8) = ga * b2(m[0][0]);
            LDS(f32x2, L_CH + 4096 + t * 8) = wc;
            LDS(f32x2, L_CH + 6144 + t * 8) = cb1;
            LDS(f32x2, L_CH + 8192 + t * 8) = sb;
            LDS(f32x2, L_CH + 10240 + t * 8) = b2(m[5][0]);
            const float s = half_sum(fold2(wc[0] + wc[1], sw[0] + sw[1]));
            if ((lane & 31) == 31) LDS(float, M_MODP + (lane >> 5) * 16 + hw * 4) = s;
        }
    } else {
        const int cw = wave - 4, tc = t - 256;
        const int chl = cw >> 1, h = cw & 1;
        u32x4 A[4][8];
        {
            const unsigned t0 = rt32();
            const unsigned mpo = a < 6 ? WS_MP + (G * 32 + j) * 1536 + cw * 384 + a * 64 + g * 16 : WS_END;
            ST16(SENT4, rs, mpo);
            const unsigned so = is_row ? (tc < 4 ? WS_R + rbk * 128 + tc * 16 : WS_END) : ((f < 7 && tc < 32) ? WS_ACT + (G * 7 + f) * 512 + tc * 16 : WS_END);
            ST16(SENT4, rs, (PT && (tc >> 3) == 16) ? WS_F1 + (G * 32 + j) * 128 + (tc & 7) * 16 : so);
            CFENCE();
            if (cw == 0) LDS(u32x4, L_U + (lane >> 5) * U_STRIDE + (lane & 31) * 16) = (u32x4){0, 0, 0, 0};
            if (cw == 1) LDS(unsigned, M_CAND + lane * 4) = 0u;
            const auto rk = __builtin_amdgcn_make_buffer_rsrc((void*)(ssmk + (ch0 + chl) * S), 0, S * 2, 0x00020000);
            unsigned ko = g * 512 + h * 256 + a * 16, ko2 = (lane >> 2) * 512 + h * 256 + (lane & 3) * 16 - 32;
            unsigned wo = (16 * j + (lane >> 2)) * 6144 + 768 * G + 192 * cw + (lane & 3) * 16;
            PIN(ko); PIN(ko2); PIN(wo);
            if (DK) wait_until(t0 + DK);
            CFENCE();
            const u32x4 k0 = __builtin_amdgcn_raw_buffer_load_b128(rk, ko, 0, 0);
            const u32x4 k1 = __builtin_amdgcn_raw_buffer_load_b128(rk, ko + 2048, 0, 0);
            const u32x4 k2 = __builtin_amdgcn_raw_buffer_load_b128(rk, ko2, 0, 0);
            const u32x2 cv = GLD(u32x2, cin, 32 * j + 8 * g);
            const u32x4 m0 = GLD(u32x4, wmod, wo), m1 = GLD(u32x4, wmod, wo + 64), m2 = GLD(u32x4, wmod, wo + 128);
            CFENCE();
            VMW(7);
            tag = mktag(rfl(ep));
            if (lds_arrive<M_CNT0>() == 7u) ST4(tag, rs, lane == 0 ? WS_F1 + (G * 32 + j) * 128 : WS_END);
            KEEP(cpf);
            STAMP(1);
            const unsigned kc = L_KC + cw * KCW;
            const unsigned w0 = kc + g * 288 + 32 + a * 16, w2 = kc + (lane >> 2) * 288 + (lane & 3) * 16;
#define SHF(k, n) ((u32x4){__builtin_amdgcn_alignbit(k[1], k[0], 16), __builtin_amdgcn_alignbit(k[2], k[1], 16), __builtin_amdgcn_alignbit(k[3], k[2], 16), __builtin_amdgcn_alignbit(n, k[3], 16)})
#define NXT(k) ((unsigned)__builtin_amdgcn_update_dpp(0, (int)k[0], 0x101, 0xf, 0xf, true))
            LDS(u32x4, w0) = k0;
            LDS(u32x4, w0 + KC1) = SHF(k0, NXT(k0));
            LDS(u32x4, w0 + 1152) = k1; LDS(u32x4, w0 + 1152 + KC1) = SHF(k1, NXT(k1));
            const u32x4 k2s = SHF(k2, NXT(k2));
            if (lane < 32 && (lane & 2) == 0) { LDS(u32x4, w2) = k2; LDS(u32x4, w2 + KC1) = k2s; }
            const u32x2 sc = {pk2(silu(blo(cv[0])), silu(bhi(cv[0]))), pk2(silu(blo(cv[1])), silu(bhi(cv[1])))};
            const unsigned ml = L_T + cw * 3072 + (lane >> 2) * 192;
            LDS(u32x4, ml + (lane & 3) * 16) = m0;
            LDS(u32x4, ml + (lane & 3) * 16 + 64) = m1;
            LDS(u32x4, ml + (lane & 3) * 16 + 128) = m2;
            STAMP(2);
            f32x4 ms = {0, 0, 0, 0};
#pragma unroll
            for (int i = 0; i < 6; ++i)
                ms = mfma16(trd(ml + (lane & 3) * 8 + i * 32), a == i ? sc : (u32x2){0u, 0u}, ms);
            ST16(__builtin_bit_cast(u32x4, ms), rs, mpo);
            CFENCE();
            STAMP(3);
            const int x0_ = 25 + a - 8 * g;
            const int cpy = x0_ & 1;
            const unsigned base = kc + cpy * KC1 + (x0_ - cpy) * 2;
#pragma unroll
            for (int mu = 0; mu < 8; ++mu)
#pragma unroll
                for (int r = 0; r < 4; ++r) {
                    const LAS unsigned* p = (const LAS unsigned*)(base + mu * 288 + r * 64);
                    A[r][mu] = (u32x4){p[0], p[1], p[2], p[3]};
                }
#pragma unroll
            for (int r = 0; r < 4; ++r)
#pragma unroll
                for (int mu = 0; mu < 8; ++mu) PIN(A[r][mu]);
        }
        unsigned base = L_U + chl * U_STRIDE + U_PADB + (16 * (a - 1) + 8 * g - h * 8 * 16 - 96) * 2;
        unsigned pw = L_P + cw * 9216 + chl * 128 + g * 288 + a * 16;
        PIN(base); PIN(pw);
        f32x4 acc[8];
#pragma unroll
        for (int i = 0; i < 8; ++i) acc[i] = (f32x4){0, 0, 0, 0};
        STAMP(4);
        __builtin_amdgcn_s_setprio(3);
        BAR();
        STAMP(5);
        u32x4 B[2][8];
#pragma unroll
        for (int tau = 0; tau < 8; ++tau) B[0][tau] = LDS(u32x4, base + (256 * tau + 96) * 2);
        SCHED();
#pragma unroll
        for (int r = 0; r < 3; ++r) {
#pragma unroll
            for (int tau = 0; tau < 8; ++tau) {
#pragma unroll
                for (int mu = 0; mu + tau < 8; ++mu) {
                    acc[mu + tau] = mfma(A[r][mu], B[r & 1][tau], acc[mu + tau]);
                    if (tau == 1 || (tau == 2 && mu == 0)) {
                        const int n = tau == 1 ? mu : 7;
                        B[(r + 1) & 1][n] = LDS(u32x4, base + (256 * n + 96 - 32 * (r + 1)) * 2);
                        SCHED();
                    }
                }
                SCHED();
            }
        }
#pragma unroll
        for (int tt = 0; tt < 8; ++tt) {
#pragma unroll
            for (int mu = 0; mu <= tt; ++mu) {
                acc[tt] = mfma(A[3][mu], B[1][tt - mu], acc[tt]);
                if (mu == tt && tt >= 2) LDS(f32x4, pw + (tt - 2) * 1152) = acc[tt - 2];
                if (tt == 7 && mu == 3) LDS(f32x4, pw + 6 * 1152) = acc[6];
                SCHED();
            }
        }
        LDS(f32x4, pw + 7 * 1152) = acc[7];
        __builtin_amdgcn_s_setprio(0);
        STAMP(6);
        BAR();
        STAMP(7);
        if (is_row) {
            yl = WS_Y + (cw * 64 + (lane >> 2)) * 8192 + rbk * 64 + (lane & 3) * 16;
            PIN(yl);
            lds_wait<M_CNT0 + 12>(1u);
            const unsigned tl = __builtin_amdgcn_readfirstlane(LDS(volatile unsigned, M_TY));
            const unsigned f4o = WS_F4 + tc * 32;
            unsigned f4;
            const bool f4now = (int)(tl + DF - rt32()) < 10;
            if (f4now) ABL("dword", f4, f4o, 0);
            {
                const unsigned xo = L_S + cw * SW + lane * 16;
                u32x4 xv[4];
                float s[4] = {0, 0, 0, 0}, q[4] = {0, 0, 0, 0};
#pragma unroll
                for (int i = 0; i < 4; ++i) xv[i] = LDS(u32x4, xo + i * 1024);
#pragma unroll
                for (int e = 0; e < 4; ++e)
#pragma unroll
                    for (int i = 0; i < 4; ++i) { s[i] = dot2(xv[i][e], 0x3f803f80u, s[i]); q[i] = dot2(xv[i][e], xv[i][e], q[i]); }
                const float mv = row_sum(fold16(fold2(s[0], s[1]), fold2(s[2], s[3]))) * (1.f / 512.f);
                const float qv = row_sum(fold16(fold2(q[0], q[1]), fold2(q[2], q[3]))) * (1.f / 512.f);
                const float var = rb(fmaxf(qv - mv * mv, 0.f));
                const float rstd = __builtin_amdgcn_rcpf(rb(__builtin_amdgcn_sqrtf(rb(var + 1e-5f))));
                if (a == 15) {
                    const unsigned lo = M_LN1 + (2 * cw + (g & 1)) * 16 + (g >> 1) * 4;
                    LDS(float, lo) = -rb(mv) * rstd;
                    LDS(float, lo + 8) = rstd;
                }
            }
            ROWPREP();
#pragma unroll
            for (int d = 0; d < 4; ++d) { tpo[d] = L_T + ((lane & 1) * 4 + d) * 2064 + (cw * 128 + (lane >> 1)) * 4; PIN(tpo[d]); }
            STAMP(22);
            if (!f4now) { wait_until(tl + DF); ABL("dword", f4, f4o, 0); }
            VMW(0); PIN(f4);
            SPIN {
                if (__builtin_expect(none(f4 != tag), 1)) break;
                STAMP(17);
                __builtin_amdgcn_s_sleep(2);
                ABL("dword", f4, f4o, 0);
                VMW(0); PIN(f4);
            }
            STAMP(8);
            wait_until(tl + D2);
            YLD();
            __builtin_amdgcn_s_setprio(1);
        }
    }
    if (is_row) {
        f32x2 gh[8];
        STAMP(9);
        BAR();
        STAMP(10);
        f32x4 wc[2];
        {
            const f32x4 st = LDS(f32x4, M_LN1 + rp * 16);
            f32x4 A1[2], A0[2];
#pragma unroll
            for (int hh = 0; hh < 2; ++hh) { A1[hh] = LDS(f32x4, cho + hh * 16); A0[hh] = LDS(f32x4, cho + 2048 + hh * 16); }
#pragma unroll
            for (int hh = 0; hh < 2; ++hh) wc[hh] = LDS(f32x4, cho + 4096 + hh * 16);
            SCHED();
            const f32x2 nm = {st[0], st[1]}, rs1 = {st[2], st[3]};
#pragma unroll
            for (int e = 0; e < 8; ++e) {
                const float a1 = A1[e >> 2][e & 3], a0 = A0[e >> 2][e & 3];
                gh[e] = pfma(pfma(x[e], rs1, nm), (f32x2){a1, a1}, (f32x2){a0, a0});
            }
#pragma unroll
            for (int e = 0; e < 8; ++e) PIN(gh[e]);
            PIN(wc[0]); PIN(wc[1]);
        }
        float Wc = 0, Sb = 0;
        if (wave == 0) {
            const f32x4 w4 = LDS(f32x4, M_MODP), s4 = LDS(f32x4, M_MODP + 16);
            Wc = (w4[0] + w4[1]) + (w4[2] + w4[3]); Sb = (s4[0] + s4[1]) + (s4[2] + s4[3]);
        }
        PIN(Wc); PIN(Sb);
        if (wave >= 4) {
            STAMP(19);
            SPIN {
                VMW(2); PIN(y[0]); PIN(y[1]);
#pragma unroll
                for (int d = 0; d < 4; ++d) { LDS(unsigned, tpo[d]) = y[0][d]; LDS(unsigned, tpo[d] + 128) = y[1][d]; }
                const unsigned mx = max4(y[0], max4(y[1], 0u));
                VMW(0); PIN(y[2]); PIN(y[3]);
#pragma unroll
                for (int d = 0; d < 4; ++d) { LDS(unsigned, tpo[d] + 256) = y[2][d]; LDS(unsigned, tpo[d] + 384) = y[3][d]; }
                if (__builtin_expect(none(max4(y[2], max4(y[3], mx)) == SENT), 1)) break;
                STAMP(21);
                __builtin_amdgcn_s_sleep(2);
                YLD();
            }
            VMW(0);
            __builtin_amdgcn_s_setprio(0);
        }
        STAMP(11);
        BAR();
        STAMP(20);
        float mv, rstd;
        {
            const u32x4 ya = LDS(u32x4, tro), yc = LDS(u32x4, tro + 16);
            f32x2 s1, s2, s3;
#pragma unroll
            for (int e = 0; e < 8; ++e) {
                x[e] = pfma(gh[e], b2(e < 4 ? ya[e & 3] : yc[e & 3]), x[e]);
                const float w = wc[e >> 2][e & 3];
                if (e == 0) { s1 = x[0]; s2 = x[0] * x[0]; s3 = x[0] * w; }
                else { s1 += x[e]; s2 = pfma(x[e], x[e], s2); s3 = pfma(x[e], (f32x2){w, w}, s3); }
            }
            const float v1 = half_sum(fold2(s1[0], s1[1])), v2 = half_sum(fold2(s2[0], s2[1])), v3 = half_sum(fold2(s3[0], s3[1]));
            if ((lane & 31) == 31) { LDS(float, ko) = v1; LDS(float, ko + 64) = v2; LDS(float, ko + 128) = v3; }
        }
        BAR();
        {
            mv = LDS(float, kr) * (1.f / 512.f);
            const float q = LDS(float, kr + 64) * (1.f / 512.f);
            const float s = LDS(float, kr + 128);
            const float var = rb(fmaxf(q - mv * mv, 0.f));
            rstd = __builtin_amdgcn_rcpf(rb(__builtin_amdgcn_sqrtf(rb(var + 1e-5f))));
            if (wave == 0) {
                const float r = rstd * (s - mv * Wc) + Sb;
                const int u = (int)(pk2(r, r) & 0xffff0000u);
                const unsigned ord = ((unsigned)u ^ ((unsigned)(u >> 31) | 0x80000000u)) >> 16;
                ST4((ord << 12) + kb_, rs, kg);
            }
        }
        CFENCE();
        STAMP(12);
        const auto ro = __builtin_amdgcn_make_buffer_rsrc(out, 0, S * D * 2, 0x00020000);
        {
            const float nmv = -mv * rstd;
            const f32x2 r2 = {rdl(rstd, 2 * rp), rdl(rstd, 2 * rp + 1)}, n2 = {rdl(nmv, 2 * rp), rdl(nmv, 2 * rp + 1)};
            f32x2 h2[8];
            f32x4 gb[2];
#pragma unroll
            for (int hh = 0; hh < 2; ++hh) {
                const f32x4 cb1 = LDS(f32x4, cho + 6144 + hh * 16), sb = LDS(f32x4, cho + 8192 + hh * 16);
                gb[hh] = LDS(f32x4, cho + 10240 + hh * 16);
#pragma unroll
                for (int e4 = 0; e4 < 4; ++e4)
                    h2[hh * 4 + e4] = pfma(pfma(x[hh * 4 + e4], r2, n2), (f32x2){cb1[e4], cb1[e4]}, (f32x2){sb[e4], sb[e4]});
            }
            const unsigned oo = (rbk * 16 + 2 * rp) * 1024 + lane * 16;
            const unsigned ho = WS_H + (rbk * 8 + rp) * 3072 + lane * 16;
#pragma unroll
            for (int i = 0; i < 2; ++i) {
                const unsigned d2 = pk2(h2[4][i], h2[5][i]);
                ST16(((u32x4){pk2(h2[0][i], h2[1][i]), __builtin_amdgcn_perm(tag, d2, 0x05040100u), pk2(h2[2][i], h2[3][i]), __builtin_amdgcn_perm(tag, d2, 0x07060302u)}), rs, ho + i * 1024);
            }
            ST16(((u32x4){pk2(h2[6][0], h2[7][0]), tag, pk2(h2[6][1], h2[7][1]), tag}), rs, ho + 2048);
#pragma unroll
            for (int e = 0; e < 8; ++e) { const float gv = gb[e >> 2][e & 3]; x[e] = pfma(h2[e], (f32x2){gv, gv}, x[e]); }
#pragma unroll
            for (int i = 0; i < 2; ++i)
                ST16(((u32x4){pk2(x[0][i], x[1][i]), pk2(x[2][i], x[3][i]), pk2(x[4][i], x[5][i]), pk2(x[6][i], x[7][i])}), ro, oo + i * 1024);
        }
        DRAIN();
        BAR();
        if (t == 0) ST4(tag, rs, WS_F3 + rbk * 128);
        STAMP(13);
    } else {
        if (f >= 7) {
            const unsigned pd = GLD(unsigned, wd, min(200 * (f - 7) + min(t, 199), F - 1) * (D * 2) + 128 * G);
            KEEP(pd);
            if (f >= 11) { STAMP_OUT(); return; }
        }
        const bool is_gu = f < 7;
        u32x4 Wf[8];
        {
            const unsigned wl = L_W + wave * 8192;
            if (is_gu) {
                const u16* wp = wave < 4 ? wg : wu;
                const unsigned wo = ((wave & 3) * 128 + (lane >> 2)) * (F * 2) + (224 * G + 32 * f + 8 * (lane & 3)) * 2;
                u32x4 W[8];
#pragma unroll
                for (int i = 0; i < 8; ++i) W[i] = GLD(u32x4, wp, wo + i * 16 * (F * 2));
#pragma unroll
                for (int i = 0; i < 8; ++i) LDS(u32x4, wl + i * 1024 + lane * 16) = W[i];
                const unsigned rp = wl + (8 * g + (a >> 2)) * 64 + (lane & 3) * 8;
#pragma unroll
                for (int c = 0; c < 2; ++c)
#pragma unroll
                    for (int ks = 0; ks < 4; ++ks) Wf[c * 4 + ks] = trd2(rp + ks * 2048 + c * 32, 256);
            } else {
                const unsigned wo = (wave * 224 + (lane >> 1)) * (D * 2) + (64 * G + 16 * (f - 7) + 8 * (lane & 1)) * 2;
                u32x4 W[7];
#pragma unroll
                for (int i = 0; i < 7; ++i) W[i] = GLD(u32x4, wd, wo + i * 32 * (D * 2));
#pragma unroll
                for (int i = 0; i < 7; ++i) LDS(u32x4, wl + i * 1024 + lane * 16) = W[i];
                const unsigned rp = wl + (8 * g + (a >> 2)) * 32 + (lane & 3) * 8;
#pragma unroll
                for (int ks = 0; ks < 7; ++ks) Wf[ks] = trd2(rp + ks * 1024, 128);
                Wf[7] = Wf[0];
            }
#pragma unroll
            for (int i = 0; i < 8; ++i) PIN(Wf[i]);
        }
        STAMP(9);
        unsigned ro_ = WS_R + (t >> 2) * 128 + (t & 3) * 16, cko = M_CAND + (t >> 3) * 4, cco = M_CAND + (t & 7) * 32, xw = L_X + wave * 1040 + lane * 16;
        unsigned hb = WS_H + lane * 16, xrd = L_X + (a & 7) * 1040 + (wave & 3) * 256 + g * 16;
        unsigned pwo = is_gu ? L_P + (wave * 8 + a) * 144 + g * 16 : L_P + (wave * 8 + a) * 80 + g * 16;
        PIN(ro_); PIN(cko); PIN(cco); PIN(xw); PIN(xrd); PIN(hb); PIN(pwo);
        {
            u32x2 fl;
            SPIN {
                CFENCE();
                fl = LD8(rs, lane < 8 ? WS_F2 + (G * 8 + lane) * 128 : WS_END);
                if ((__builtin_amdgcn_ballot_w64(fl[0] != tag) & 0xffull) == 0ull) break;
                __builtin_amdgcn_s_sleep(4);
            }
            const unsigned now = rt32();
            const unsigned tl = wave_max(lane < 8 ? fl[1] - now + 0x40000000u : 0u) - 0x40000000u + now;
            wait_until(tl + D3);
        }
        STAMP(10);
        {
            u32x4 key;
            unsigned lm;
            SPIN {
                CFENCE();
                key = LD16(rs, ro_);
                lm = max4(key, 0u);
                if (none(lm == SENT)) break;
            }
            STAMP(11);
            const unsigned wm_ = wave_max_v(lm);
            if (lane == 63) LDS(unsigned, M_WMAX + wave * 4) = wm_;
            const unsigned a0 = max(key[0], key[1]), a1 = min(key[0], key[1]), a2 = max(key[2], key[3]), a3 = min(key[2], key[3]);
            const unsigned m1 = min(a0, a2), m2 = max(a1, a3);
            unsigned k0 = lm, k1 = max(m1, m2), k2 = min(m1, m2), k3 = min(a1, a3);
            BAR();
            const u32x4 ma = LDS(u32x4, M_WMAX), mb = LDS(u32x4, M_WMAX + 16);
            unsigned T = min(min(min(ma[0], ma[1]), min(ma[2], ma[3])), min(min(mb[0], mb[1]), min(mb[2], mb[3])));
            const u64 b0 = __builtin_amdgcn_ballot_w64(k0 >= T), b1 = __builtin_amdgcn_ballot_w64(k1 >= T);
            const int n0 = __builtin_popcountll(b0);
            if (__builtin_amdgcn_ballot_w64(k2 >= T) == 0ull && n0 + __builtin_popcountll(b1) <= 8) {
                const int pos = __builtin_amdgcn_mbcnt_hi((unsigned)(b0 >> 32), __builtin_amdgcn_mbcnt_lo((unsigned)b0, wave * 8));
                if (k0 >= T) LDS(unsigned, M_CAND + pos * 4) = k0;
                if (b1 != 0ull) {
                    const int pos1 = __builtin_amdgcn_mbcnt_hi((unsigned)(b1 >> 32), __builtin_amdgcn_mbcnt_lo((unsigned)b1, wave * 8 + n0));
                    if (k1 >= T) LDS(unsigned, M_CAND + pos1 * 4) = k1;
                }
            } else {
#pragma unroll 1
                for (int i = 0; i < 8; ++i) {
                    const unsigned mx = wave_max(k0);
                    if (mx < __builtin_amdgcn_readfirstlane(T)) break;
                    if (k0 == mx) { LDS(unsigned, M_CAND + (wave * 8 + i) * 4) = mx; k0 = k1; k1 = k2; k2 = k3; k3 = 0u; }
                }
            }
            BAR();
            {
                const unsigned kx = LDS(unsigned, cko);
                const u32x4 c0 = LDS(u32x4, cco), c1 = LDS(u32x4, cco + 16);
                unsigned bits = 0;
#pragma unroll
                for (int e = 0; e < 4; ++e) bits = __builtin_amdgcn_alignbit(bits, kx - c0[e], 31);
#pragma unroll
                for (int e = 0; e < 4; ++e) bits = __builtin_amdgcn_alignbit(bits, kx - c1[e], 31);
                int rank = __builtin_popcount(bits);
                rank += __builtin_amdgcn_update_dpp(0, rank, 0xb1, 0xf, 0xf, true);
                rank += __builtin_amdgcn_update_dpp(0, rank, 0x4e, 0xf, 0xf, true);
                rank += __builtin_amdgcn_update_dpp(0, rank, 0x141, 0xf, 0xf, true);
                if (rank < 8) LDS(unsigned, M_SEL + rank * 4) = kx;
            }
            BAR();
        }
        STAMP(12);
        if (is_gu) {
            const unsigned srow = ~__builtin_amdgcn_readfirstlane(LDS(unsigned, M_SEL + wave * 4)) & 0x7ffu;
            const unsigned so2 = (srow >> 1) * 3072 + 2048 + (srow & 1) * 8, so1 = (srow >> 1) * 3072 + (srow & 1) * 1024;
            SPIN {
                u32x4 hv; u32x2 hw_;
                ABLS("dwordx4", hv, hb, so1); ABLS("dwordx2", hw_, hb, so2);
                VMW(0); PIN(hv); PIN(hw_);
                LDS(u32x4, xw) = (u32x4){hv[0], hv[2], __builtin_amdgcn_perm(hv[3], hv[1], 0x05040100u), hw_[0]};
                if (__builtin_expect(none(((__builtin_amdgcn_perm(hv[3], hv[1], 0x07060302u) ^ tag) | (hw_[1] ^ tag)) != 0u), 1)) break;
            }
            STAMP(13);
            BAR();
            u32x4 xa[4];
#pragma unroll
            for (int ks = 0; ks < 4; ++ks) xa[ks] = LDS(u32x4, xrd + ks * 64);
            f32x4 acc0 = {0, 0, 0, 0}, acc1 = {0, 0, 0, 0};
#pragma unroll
            for (int ks = 0; ks < 4; ++ks) { acc0 = mfma(Wf[ks], xa[ks], acc0); acc1 = mfma(Wf[4 + ks], xa[ks], acc1); }
            if (a < 8) { LDS(f32x4, pwo) = acc0; LDS(f32x4, pwo + 64) = acc1; }
            BAR();
            if (wave < 4) {
                const unsigned pp = L_P + (lane >> 3) * 144 + (wave * 8 + (lane & 7)) * 4;
                float v[8];
#pragma unroll
                for (int w = 0; w < 8; ++w) v[w] = LDS(float, pp + w * 1152);
                const float gp = rb((v[0] + v[1]) + (v[2] + v[3])), up = rb((v[4] + v[5]) + (v[6] + v[7]));
                const float o = rb(silu(gp)) * up;
                const float on = __int_as_float(__builtin_amdgcn_update_dpp(0, __float_as_int(o), 0xb1, 0xf, 0xf, true));
                ST4(pk2(o, on), rs, (t & 1) ? WS_END : WS_ACT + (G * 7 + f) * 512 + t * 2);
            }
            STAMP(14);
        } else {
            const unsigned tk = rt32();
            const auto ro = __builtin_amdgcn_make_buffer_rsrc(out, 0, S * D * 2, 0x00020000);
            const int col0 = 64 * G + 16 * (f - 7);
            float o0 = 0, o1 = 0, g0 = 0, g1 = 0;
            unsigned ooff = 0;
            if (wave == 0) {
                const unsigned fk = LDS(unsigned, M_SEL + (lane >> 3) * 4);
                const int frow = 2047 - (int)(fk & 0x7ffu);
                unsigned u = (fk >> 12) ^ 0x8000u;
                u = (u & 0x8000u) ? (u ^ 0x7fffu) : u;
                const float sig = rb(__builtin_amdgcn_rcpf(1.f + __expf(-blo(u))));
                u32x2 gbv;
                SPIN {
                    CFENCE();
                    gbv = LD8(rs, WS_MOD + (1280 + col0 / 2 + (lane & 7)) * 8);
                    const unsigned v = LD4(rs, WS_F3 + (frow >> 4) * 128);
                    if (none(((v ^ tag) | (gbv[1] ^ tag)) != 0u)) break;
                    __builtin_amdgcn_s_sleep(2);
                }
                CFENCE();
                ooff = (frow * D + col0 + 2 * (lane & 7)) * 2;
                const unsigned oprev = LD4(ro, ooff);
                o0 = blo(oprev); o1 = bhi(oprev); g0 = blo(gbv[0]) * sig; g1 = bhi(gbv[0]) * sig;
                PIN(o0); PIN(o1); PIN(g0); PIN(g1);
            }
            unsigned ao = a < 8 ? WS_ACT + wave * 7 * 512 + g * 128 + a * 16 : WS_END;
            unsigned pp = L_P + (lane >> 3) * 80 + (lane & 7) * 8;
            PIN(ao); PIN(pp);
            wait_until(tk + D4);
            u32x4 av[7];
            SPIN {
                CFENCE();
#pragma unroll
                for (int ks = 0; ks < 7; ++ks) av[ks] = LD16(rs, ao + ks * 512);
                unsigned m = 0;
#pragma unroll
                for (int ks = 0; ks < 7; ++ks) m = max4(av[ks], m);
                if (none(m == SENT)) break;
                __builtin_amdgcn_s_sleep(1);
            }
            STAMP(13);
            f32x4 acc0 = {0, 0, 0, 0};
#pragma unroll
            for (int ks = 0; ks < 7; ++ks) acc0 = mfma(Wf[ks], av[ks], acc0);
            if (a < 8) LDS(f32x4, pwo) = acc0;
            BAR();
            if (wave == 0) {
                f32x2 v[8];
#pragma unroll
                for (int w = 0; w < 8; ++w) v[w] = LDS(f32x2, pp + w * 640);
                const f32x2 s = ((v[0] + v[1]) + (v[2] + v[3])) + ((v[4] + v[5]) + (v[6] + v[7]));
                ST4(pk2(o0 + g0 * rb(s[0]), o1 + g1 * rb(s[1])), ro, ooff);
                if (G == 0 && f == 7 && lane == 0) ST4(tag, rs, WS_E);
            }
            STAMP(14);
        }
    }
    STAMP_OUT();
}

extern "C" void kernel_launch(void* const* d_in, const int* in_sizes, int n_in,
                              void* d_out, int out_size, void* d_ws, size_t ws_size,
                              hipStream_t stream) {
    fused_kernel<<<256, 512, 0, stream>>>((const u16*)d_in[1], (const u16*)d_in[2], (char*)d_ws, (const u16*)d_in[4],
                                          (const u16*)d_in[3], (const u16*)d_in[11], (const u16*)d_in[12], (const u16*)d_in[5],
                                          (const u16*)d_in[0], (const u16*)d_in[7], (const u16*)d_in[8], (const u16*)d_in[9],
                                          (const u16*)d_in[10], (u16*)d_out);
}
```

```cpp
#include <hip/hip_runtime.h>
#include <stdint.h>
#pragma clang diagnostic ignored "-Wint-to-pointer-cast"

typedef unsigned short u16;
typedef unsigned long long u64;
typedef short bf16x8 __attribute__((ext_vector_type(8)));
typedef short s16x4 __attribute__((ext_vector_type(4)));
typedef __bf16 bf2 __attribute__((ext_vector_type(2)));
typedef float f32x4 __attribute__((ext_vector_type(4)));
typedef float f32x2 __attribute__((ext_vector_type(2)));
typedef unsigned u32x4 __attribute__((ext_vector_type(4)));
typedef unsigned u32x2 __attribute__((ext_vector_type(2)));
#define LAS __attribute__((address_space(3)))
#define GAS __attribute__((address_space(1)))

constexpr int S = 2048, D = 512, F = 1792;
constexpr unsigned SENT = 0xFFFFFFFFu;

constexpr unsigned WS_E = 0;
constexpr unsigned WS_F1 = 1024;
constexpr unsigned WS_F2 = WS_F1 + 256 * 128;
constexpr unsigned WS_F4 = WS_F2 + 64 * 128;
constexpr unsigned WS_F3 = WS_F4 + 256 * 128;
constexpr unsigned WS_MOD = WS_F3 + 128 * 128;
constexpr unsigned WS_R = WS_MOD + 12288;
constexpr unsigned WS_ACT = WS_R + 16384;
constexpr unsigned WS_MP = 196608;
constexpr unsigned WS_U = WS_MP + 393216;
constexpr unsigned WS_Y = WS_U + 3145728 + 4096;
constexpr unsigned WS_H = WS_Y + 2097152 + 4096;
constexpr unsigned WS_END = WS_H + 3145728 + 4096;
constexpr unsigned WS_DBG = WS_END;
static_assert(WS_ACT + 28672 <= WS_MP, "ws layout");

constexpr int U_PADB = 512, U_STRIDE = 4672;
constexpr int L_U = 0;
constexpr int L_P = 9344;
constexpr int L_MISC = L_P + 4 * 9216 + 128;
constexpr int L_S = L_MISC + 2048;
constexpr int SW = 36 * 128;
constexpr int KC1 = 2368, KCW = 4736;
constexpr int L_KC = L_S + 32768;
constexpr int L_T = L_KC + 4 * KCW;
constexpr int L_CH = L_T + 8 * 2064;
constexpr int L_X = L_S;
constexpr int L_W = L_S;
constexpr int L_END = L_CH + 12288;
static_assert(L_W + 65536 <= L_CH, "lds layout");
static_assert(L_END <= 160 * 1024 && L_END > 80 * 1024, "lds size");
constexpr int M_WMAX = L_MISC;
constexpr int M_SEL = L_MISC + 64;
constexpr int M_REDO = L_MISC + 120;
constexpr int M_TY = L_MISC + 112;
constexpr int M_CNT0 = L_MISC + 96;
constexpr int M_LN1 = L_MISC + 384;
constexpr int M_SUMS = L_MISC + 128;
constexpr int M_CAND = L_MISC + 768;
constexpr int M_DUMMY = L_MISC + 1280;
constexpr int M_SEL2 = L_MISC + 1024;
constexpr int M_DUMP = L_U;
constexpr int M_MODP = L_MISC + 512;

#define CPF_LANE (t - 256)
#define CODE_LINES 156
#define PFW 1
#define DP 32
#define DW 128
#define DF 160
#define TOUCH 0
#define PDW 0
#define DE 140
#define F4S 32
#define UFL 1
#define ODEL 16
#define TH 0
#define DU 0
#define PS 0
#define PM 0
#define DA 0
#define PT 1
#define DO 10
#define DK 0
#define DM 110
#define D2 235
#define D3 0
#define D4 105
constexpr unsigned SPIN_CAP = 40000;

__device__ __forceinline__ float blo(unsigned w) { return __uint_as_float(w << 16); }
__device__ __forceinline__ float bhi(unsigned w) { return __uint_as_float(w & 0xffff0000u); }
__device__ __forceinline__ f32x2 b2(unsigned w) { return (f32x2){blo(w), bhi(w)}; }
__device__ __forceinline__ float rb(float f) { return (float)(__bf16)f; }
__device__ __forceinline__ unsigned pk2(float lo, float hi) {
    bf2 v; v[0] = (__bf16)lo; v[1] = (__bf16)hi;
    return __builtin_bit_cast(unsigned, v);
}
__device__ __forceinline__ float dot2(unsigned a, unsigned b, float c) {
    return __builtin_amdgcn_fdot2_f32_bf16(__builtin_bit_cast(bf2, a), __builtin_bit_cast(bf2, b), c, false);
}
__device__ __forceinline__ f32x2 pfma(f32x2 a, f32x2 b, f32x2 c) { return __builtin_elementwise_fma(a, b, c); }
__device__ __forceinline__ unsigned rt32() { return (unsigned)__builtin_amdgcn_s_memrealtime(); }
#define BAR() do { asm volatile("s_waitcnt lgkmcnt(0)" ::: "memory"); __builtin_amdgcn_s_barrier(); asm volatile("" ::: "memory"); } while (0)
#define DRAIN() asm volatile("s_waitcnt vmcnt(0)" ::: "memory")
#define CFENCE() asm volatile("" ::: "memory")
#define PIN(x) asm volatile("" : "+v"(x))
#define PINS(x) asm volatile("" : "+s"(x))
#define KEEP(x) asm volatile("" :: "v"(x))
#define SCHED() __builtin_amdgcn_sched_barrier(0)
#define SPIN _Pragma("unroll 1") for (unsigned sp_ = 0; sp_ < SPIN_CAP; ++sp_)
#define LDS(T, off) (*(LAS T*)(unsigned)(off))
template <int OFF>
__device__ __forceinline__ unsigned lds_arrive() {
    unsigned old; u64 ex;
    asm volatile("s_mov_b64 %1, exec\n\ts_mov_b64 exec, 1\n\tds_add_rtn_u32 %0, %2, %3 offset:%4\n\ts_mov_b64 exec, %1\n\ts_waitcnt lgkmcnt(0)"
                 : "=&v"(old), "=&s"(ex) : "v"(0u), "v"(1u), "i"(OFF) : "memory");
    return __builtin_amdgcn_readfirstlane(old);
}
template <int OFF>
__device__ __forceinline__ void lds_signal() {
    u64 ex;
    asm volatile("s_mov_b64 %0, exec\n\ts_mov_b64 exec, 1\n\tds_add_u32 %1, %2 offset:%3\n\ts_mov_b64 exec, %0"
                 : "=&s"(ex) : "v"(0u), "v"(1u), "i"(OFF) : "memory");
}
template <int OFF>
__device__ __forceinline__ void lds_wait(unsigned want) {
#pragma unroll 1
    for (unsigned sp_ = 0; sp_ < 4000000u; ++sp_) { CFENCE(); if (__builtin_amdgcn_readfirstlane(LDS(volatile unsigned, OFF)) >= want) break; }
    CFENCE();
}
__device__ __forceinline__ unsigned mktag(unsigned e) { return e >= SENT - 1u ? 1u : e + 1u; }
__device__ __forceinline__ unsigned rfl(unsigned v) { unsigned r; asm volatile("v_readfirstlane_b32 %0, %1" : "=s"(r) : "v"(v)); return r; }
__device__ __forceinline__ float smul(float a, float b) { float r; asm("v_mul_f32 %0, %1, %2" : "=v"(r) : "v"(a), "v"(b)); return r; }
__device__ __forceinline__ float sadd(float a, float b) { float r; asm("v_add_f32 %0, %1, %2" : "=v"(r) : "v"(a), "v"(b)); return r; }

template <int CTRL, int RMASK = 0xf>
__device__ __forceinline__ float dpp_add(float v) {
    return v + __int_as_float(__builtin_amdgcn_update_dpp(0, __float_as_int(v), CTRL, RMASK, 0xf, true));
}
__device__ __forceinline__ float half_sum(float v) {
    v = dpp_add<0x111>(v); v = dpp_add<0x112>(v); v = dpp_add<0x114>(v); v = dpp_add<0x118>(v);
    return dpp_add<0x142>(v);
}
__device__ __forceinline__ float fold2(float a, float b) {
    auto r = __builtin_amdgcn_permlane32_swap(__float_as_uint(a), __float_as_uint(b), false, false);
    return __uint_as_float(r[0]) + __uint_as_float(r[1]);
}
__device__ __forceinline__ float fold16(float a, float b) {
    auto r = __builtin_amdgcn_permlane16_swap(__float_as_uint(a), __float_as_uint(b), false, false);
    return __uint_as_float(r[0]) + __uint_as_float(r[1]);
}
__device__ __forceinline__ float row_sum(float v) {
    v = dpp_add<0x111>(v); v = dpp_add<0x112>(v); v = dpp_add<0x114>(v); return dpp_add<0x118>(v);
}
__device__ __forceinline__ float rdl(float v, int l) { return __int_as_float(__builtin_amdgcn_readlane(__float_as_int(v), l)); }
template <int CTRL, int RMASK = 0xf>
__device__ __forceinline__ unsigned dpp_max(unsigned v) {
    return max(v, (unsigned)__builtin_amdgcn_update_dpp(0, (int)v, CTRL, RMASK, 0xf, true));
}
__device__ __forceinline__ unsigned wave_max_v(unsigned v) {
    v = dpp_max<0x111>(v); v = dpp_max<0x112>(v); v = dpp_max<0x114>(v); v = dpp_max<0x118>(v);
    v = dpp_max<0x142>(v); v = dpp_max<0x143>(v);
    return v;
}
__device__ __forceinline__ unsigned row_allmax(unsigned v) {
    v = dpp_max<0x128>(v); v = dpp_max<0x124>(v); v = dpp_max<0x122>(v); return dpp_max<0x121>(v);
}
__device__ __forceinline__ unsigned row_max(unsigned v) {
    v = dpp_max<0x111>(v); v = dpp_max<0x112>(v); v = dpp_max<0x114>(v); v = dpp_max<0x118>(v);
    return (unsigned)__builtin_amdgcn_readlane((int)v, 15);
}
__device__ __forceinline__ unsigned wave_max(unsigned v) { return (unsigned)__builtin_amdgcn_readlane((int)wave_max_v(v), 63); }
__device__ __forceinline__ u32x2 trd(unsigned off) {
    return __builtin_bit_cast(u32x2, __builtin_amdgcn_ds_read_tr16_b64_v4i16((LAS s16x4*)off));
}
__device__ __forceinline__ u32x4 trd2(unsigned off, unsigned d) { const u32x2 lo = trd(off), hi = trd(off + d); return (u32x4){lo[0], lo[1], hi[0], hi[1]}; }
__device__ __forceinline__ f32x4 mfma(u32x4 a, u32x4 b, f32x4 c) {
    return __builtin_amdgcn_mfma_f32_16x16x32_bf16(__builtin_bit_cast(bf16x8, a), __builtin_bit_cast(bf16x8, b), c, 0, 0, 0);
}
__device__ __forceinline__ f32x4 mfma16(u32x2 a, u32x2 b, f32x4 c) {
    return __builtin_amdgcn_mfma_f32_16x16x16bf16_1k(__builtin_bit_cast(s16x4, a), __builtin_bit_cast(s16x4, b), c, 0, 0, 0);
}
__device__ __forceinline__ float silu(float x) { return x * __builtin_amdgcn_rcpf(1.f + __expf(-x)); }
__device__ __forceinline__ unsigned max3u(unsigned a, unsigned b, unsigned c) { return max(max(a, b), c); }
__device__ __forceinline__ unsigned max4(u32x4 v, unsigned m) { return max3u(max3u(v[0], v[1], m), v[2], v[3]); }
__device__ __forceinline__ bool none(bool c) { return __builtin_amdgcn_ballot_w64(c) == 0ull; }
__device__ __forceinline__ void wait_until(unsigned t) {
#pragma unroll 1
    while (t - 1u - rt32() < 1024u) {}
}

#define STAMP(i) do {} while (0)
#define STAMP_OUT() do {} while (0)

#define LD16(rs, off) __builtin_amdgcn_raw_buffer_load_b128(rs, off, 0, 16)
#define LD8(rs, off) __builtin_amdgcn_raw_buffer_load_b64(rs, off, 0, 16)
#define LD4(rs, off) __builtin_amdgcn_raw_buffer_load_b32(rs, off, 0, 16)
#define ST16(v, rs, off) __builtin_amdgcn_raw_buffer_store_b128(v, rs, off, 0, 16)
#define ST8(v, rs, off) __builtin_amdgcn_raw_buffer_store_b64(v, rs, off, 0, 16)
#define ST4(v, rs, off) __builtin_amdgcn_raw_buffer_store_b32(v, rs, off, 0, 16)
#define ABL(op, d, off, imm) asm volatile("buffer_load_" op " %0, %1, %2, 0 offen offset:%3 sc1" : "=v"(d) : "v"(off), "s"(rq), "i"(imm) : "memory")
#define ABLS(op, d, off, soff) asm volatile("buffer_load_" op " %0, %1, %2, %3 offen sc1" : "=v"(d) : "v"(off), "s"(rq), "s"(soff) : "memory")
#define AGL(op, d, p, off, imm) asm volatile("global_load_" op " %0, %1, %2 offset:%3" : "=v"(d) : "v"(off), "s"(p), "i"(imm) : "memory")
#define VMW(n) asm volatile("s_waitcnt vmcnt(" #n ")" ::: "memory")
#define SENT4 ((u32x4){SENT, SENT, SENT, SENT})
#define GLD(T, p, off) (*(const GAS T*)((const GAS char*)(p) + (off)))

__global__ void __launch_bounds__(512, 2)
fused_kernel(const u16* __restrict__ kin, const u16* __restrict__ vin, char* __restrict__ ws, const u16* __restrict__ ssmk,
             const u16* __restrict__ shk, const u16* __restrict__ cin, const u16* __restrict__ wmod, const u16* __restrict__ ssmD,
             const u16* __restrict__ xq, const u16* __restrict__ wr, const u16* __restrict__ wg, const u16* __restrict__ wu,
             const u16* __restrict__ wd, u16* __restrict__ out) {
    __shared__ __attribute__((aligned(16))) char lds_[L_END];
    asm volatile("" :: "v"((LAS char*)lds_) : "memory");
    STAMP(0);
    int b = blockIdx.x;
    const int t = threadIdx.x, lane = t & 63;
    int wave = __builtin_amdgcn_readfirstlane(t >> 6);
    unsigned cpf;
    u32x2 shw;
    {
        const unsigned toff = (b >> 3) * 65536 + (b & 7) * 128 + wave * 16384 + (lane >> 3) * 1024 + (((lane & 7) * 16) ^ ((lane & 16) * 4));
        u64 pc; asm volatile("s_getpc_b64 %0" : "=s"(pc));
        if (wave < 4) {
            asm volatile("s_mov_b32 m0, %7\n\ts_nop 0\n\tglobal_load_lds_dwordx4 %0, %5\n\tglobal_load_lds_dwordx4 %1, %5 offset:1024\n\t"
                         "global_load_lds_dwordx4 %2, %5 offset:1536\n\ts_sub_u32 m0, m0, 0x800\n\ts_nop 0\n\t"
                         "global_load_lds_dwordx4 %3, %6\n\tglobal_load_lds_dwordx4 %4, %6 offset:1024"
                         :: "v"(__builtin_elementwise_sub_sat(toff, 4096u)), "v"(toff + 3072u), "v"(toff + 6656u), "v"(toff), "v"(toff + 7168u),
                            "s"(kin), "s"(vin), "s"(L_S + 2048 + wave * SW) : "memory", "scc");
            AGL("dwordx2", shw, shk, (b & 7) * 512 + lane * 8, 0);
            __builtin_amdgcn_s_barrier();
        } else {
            AGL("dword", cpf, pc & ~(u64)255, min((b >> 3) + 32 * min(CPF_LANE, 4), CODE_LINES - 1) * 128, 0);
            LDS(u32x4, M_CNT0) = (u32x4){0, 0, 0, 0};
            BAR();
        }
        CFENCE();
    }
    PINS(b); PINS(wave);
    PINS(ws); PINS(ssmk); PINS(cin); PINS(wmod); PINS(ssmD);
    PINS(xq); PINS(wr); PINS(wg); PINS(wu); PINS(wd); PINS(out);
    unsigned ep;
    AGL("dword", ep, ws, 0u, WS_E);
    const int G = b & 7, j = b >> 3;
    const int ch0 = 64 * G + 2 * j;
    const auto rs = __builtin_amdgcn_make_buffer_rsrc(ws, 0, WS_END, 0x00020000);
    const u32x4 rq = {(unsigned)(u64)ws, (unsigned)((u64)ws >> 32) & 0xffffu, WS_END, 0x00020000u};
    STAMP(15);
    const bool is_row = j < 16;
    const int f = j - 16;
    const int rbk = G * 16 + j;
    const int a = lane & 15, g = lane >> 4;
    unsigned tag;
    u32x4 y[4];
    unsigned yl;
#define YLD() do { ABL("dwordx4", y[0], yl, 0); ABLS("dwordx4", y[1], yl, 131072u); ABLS("dwordx4", y[2], yl, 262144u); ABLS("dwordx4", y[3], yl, 393216u); } while (0)
    const int rp = 2 * (wave & 3) + (wave >> 2);
    unsigned tro, kb_, ko, kr, kg, cho;
    f32x2 x[8];
    unsigned xo_;
#define ROWADDR() do { \
        tro = L_T + rp * 2064 + lane * 32; kb_ = 0x800u + rbk * 16 + a; ko = M_SUMS + rp * 8 + (lane >> 5) * 4; kr = M_SUMS + a * 4; \
        kg = lane < 16 ? WS_R + rbk * 128 + lane * 4 : WS_END; cho = L_CH + lane * 32; xo_ = L_S + (wave & 3) * SW + lane * 16; \
        PIN(tro); PIN(kb_); PIN(ko); PIN(kr); PIN(kg); PIN(cho); PIN(xo_); } while (0)
#define XUNPACK(x0_, x1_) do { \
        _Pragma("unroll") for (int e = 0; e < 8; ++e) x[e] = (e & 1) ? (f32x2){bhi(x0_[e >> 1]), bhi(x1_[e >> 1])} : (f32x2){blo(x0_[e >> 1]), blo(x1_[e >> 1])}; \
        _Pragma("unroll") for (int e = 0; e < 8; ++e) PIN(x[e]); } while (0)
    unsigned tpo[4];
    if (wave < 4) {
        const int hw = wave;
        unsigned yo = WS_Y + (G * 32 + j) * 8192 + t * 16;
        const int chl = (t >> 1) & 1, half = t & 1;
        unsigned dw, ww;
        AGL("ushort", dw, ssmD, (ch0 + chl) * 2, 0);
        AGL("dword", ww, wr, t * 4, 0);
        unsigned bo = is_row ? (rbk * 16 + 4 * hw) * 1024 + lane * 16
                             : WS_MP + (G * 32 + 4 * (lane >> 3)) * 1536 + f * 128 + (lane & 7) * 16;
        unsigned fo = lane < 8 ? WS_F1 + (G * 32 + 8 * hw + lane) * 128 : WS_END;
        unsigned ub = WS_U + (UFL ? 8 * G + (j >> 2) : 16 * G + (j >> 1)) * (128 * (TH ? 3 : 2) * (UFL ? 128 : 64)) + (hw * 32 * (TH ? 3 : 2) + (lane >> 1)) * (UFL ? 128 : 64)
                      + (j & (UFL ? 3 : 1)) * 32 + (lane & 1) * 16;
        unsigned ul = L_U + (lane & 1) * U_STRIDE + U_PADB + (hw * 64 + (lane >> 1)) * 16;
        unsigned ul3[3], ux3[3];
#pragma unroll
        for (int i = 0; i < 3; ++i) {
            const int pi = 32 * i + (lane >> 1), rg = pi / 3, pc_ = pi - 3 * rg;
            ul3[i] = L_U + (lane & 1) * U_STRIDE + U_PADB + (hw * 32 + rg) * 32 + pc_ * 8;
            ux3[i] = pc_ < 2 ? ul3[i] + 24 - pc_ * 4 : M_DUMMY + lane * 4;
        }
        {
            constexpr int NP = TH ? 3 : 2, PB = UFL ? 128 : 64;
            unsigned uo = WS_U + (UFL ? 8 * G + (lane >> 3) : 16 * G + (lane >> 2)) * (128 * NP * PB) + (4 * j + hw) * (NP * PB) + (lane & (PB / 16 - 1)) * 16;
            const int q = a >> 2;
            const unsigned cs = 32 * g + 8 * (lane & 3), sb_ = L_S + hw * SW;
#define RADDR(r) ((r) * 128 + (cs ^ (((r) & 2) << 5)))
            unsigned rv = sb_ + RADDR(q), ro1 = sb_ + 2048 + RADDR(q + 1), re2 = sb_ + 2048 + RADDR(q + 2), rl = sb_ + 2048 + RADDR(17 + q - (q >> 1));
            unsigned f1o = lane == 0 ? WS_F1 + (G * 32 + j) * 128 : WS_END;
            PIN(fo); PIN(ub);
            if (TH) { PIN(ul3[0]); PIN(ul3[1]); PIN(ul3[2]); PIN(ux3[0]); PIN(ux3[1]); PIN(ux3[2]); } else PIN(ul);
            f32x2 o[8];
#pragma unroll
            for (int i = 0; i < 8; ++i) { o[i] = (f32x2){0.f, 0.f}; PIN(o[i]); }
            PIN(rv); PIN(ro1); PIN(re2); PIN(rl); PIN(uo); PIN(yo); PIN(bo); PIN(f1o);
            ROWADDR();
            VMW(4);
            if (__builtin_expect(j == 0 && hw == 0, 0)) { if (lane < 32) LDS(u32x4, L_S + 2048 + lane * 16) = (u32x4){0, 0, 0, 0}; }
            CFENCE();
            STAMP(1);
            unsigned Q[18], V[8];
#pragma unroll
            for (int i = 0; i < 4; ++i) {
                const u32x2 qo = trd(ro1 + i * 512), qe = trd(re2 + i * 512), vv = trd(rv + i * 512);
                Q[4 * i] = qo[0]; Q[4 * i + 2] = qo[1]; Q[4 * i + 1] = qe[0]; Q[4 * i + 3] = qe[1]; V[2 * i] = vv[0]; V[2 * i + 1] = vv[1];
            }
            { const u32x2 ql = trd(rl); Q[16] = ql[0]; Q[17] = ql[1]; }
            SCHED();
            VMW(3); PIN(shw);
            const unsigned w32 = __builtin_amdgcn_alignbit(shw[1], shw[1], 16), w10 = __builtin_amdgcn_alignbit(shw[0], shw[0], 16);
            if (TH) {
#pragma unroll
                for (int i = 0; i < 16; ++i) o[i >> 1][i & 1] = dot2(Q[i + 2], w10, dot2(Q[i], w32, o[i >> 1][i & 1]));
                unsigned d[8];
#pragma unroll
                for (int i = 0; i < 8; ++i) { const f32x2 p = o[i] * b2(V[i]); d[(i & 4) + 3 - (i & 3)] = pk2(p[1], p[0]); PIN(d[(i & 4) + 3 - (i & 3)]); }
                VMW(2);
                tag = mktag(rfl(ep));
                ST16(((u32x4){d[4], tag << 16, d[5], tag}), rs, uo + 2 * PB);
                ST16(((u32x4){d[0], __builtin_amdgcn_perm(tag, d[6], 0x05040100u), d[1], __builtin_amdgcn_perm(tag, d[6], 0x07060302u)}), rs, uo);
                ST16(((u32x4){d[2], __builtin_amdgcn_perm(tag, d[7], 0x05040100u), d[3], __builtin_amdgcn_perm(tag, d[7], 0x07060302u)}), rs, uo + PB);
                CFENCE();
                STAMP(2);
            } else {
#pragma unroll
                for (int hh = 0; hh < 2; ++hh) {
#pragma unroll
                    for (int i = 8 * hh; i < 8 * hh + 8; ++i) o[i >> 1][i & 1] = dot2(Q[i + 2], w10, dot2(Q[i], w32, o[i >> 1][i & 1]));
#pragma unroll
                    for (int i = 4 * hh; i < 4 * hh + 4; ++i) o[i] *= b2(V[i]);
                    const u32x4 r = {pk2(o[4 * hh + 3][1], o[4 * hh + 3][0]), pk2(o[4 * hh + 2][1], o[4 * hh + 2][0]), pk2(o[4 * hh + 1][1], o[4 * hh + 1][0]), pk2(o[4 * hh][1], o[4 * hh][0])};
                    ST16(r, rs, uo + PB * hh);
                    SCHED();
                }
                CFENCE();
                STAMP(2);
                VMW(4);
                tag = mktag(rfl(ep));
                DRAIN();
            }
            if (lds_arrive<M_CNT0>() == 7u) ST4(tag, rs, f1o);
            STAMP(3);
        }
        const u16* pfa = hw < 2 ? wg : wu;
        unsigned pfo = (32 * f + 16 * (hw & 1) + (lane >> 2)) * (F * 2) + ((448 * G) / 128 + (lane & 3)) * 128;
        unsigned pdo = (112 * f + 28 * hw + min(lane, 27)) * (D * 2) + 128 * G;
        PIN(pdo);
        unsigned pr = L_P + chl * (2 * 9216 + 128) + hw * 1152 + half * 576 + (lane >> 2) * 16;
        unsigned kvo = L_U + chl * U_STRIDE + U_PADB + (t >> 2) * 32 + half * 16;
        unsigned mo = WS_MOD + t * 8;
        unsigned c2o = (t == 0 && j < 8) ? WS_F2 + (j * 8 + G) * 128 : WS_END;
        unsigned f2o = lane < 8 ? WS_F2 + (G * 8 + lane) * 128 : WS_END;
        PIN(pfo); PIN(pr); PIN(kvo); PIN(mo); PIN(c2o); PIN(f2o);
        const bool owner = !is_row && hw == 3 && f < 12;
        u32x4 xr[4];
#define MPLD() do { ABL("dwordx4", xr[0], bo, 0); ABL("dwordx4", xr[1], bo, 1536); ABL("dwordx4", xr[2], bo, 3072); ABLS("dwordx4", xr[3], bo, 4608u); } while (0)
#define XLD() asm volatile("s_mov_b32 m0, %2\n\ts_nop 0\n\tglobal_load_lds_dwordx4 %0, %1\n\tglobal_load_lds_dwordx4 %0, %1 offset:1024\n\t" \
                           "global_load_lds_dwordx4 %0, %1 offset:2048\n\tglobal_load_lds_dwordx4 %0, %1 offset:3072" :: "v"(bo), "s"(xq), "s"(L_S + hw * SW) : "memory")
        STAMP(4);
        if (DP) wait_until(rt32() + DP);
        SPIN {
            unsigned v;
            ABL("dword", v, fo, 0);
            VMW(0); PIN(v);
            if ((__builtin_amdgcn_ballot_w64(v != tag) & 0xffull) == 0ull) break;
            if (PM) fo = v != tag ? fo : WS_E + 256;
            if (PS) __builtin_amdgcn_s_sleep(PS);
        }
        STAMP(16);
        {
            u32x4 u[3];
            constexpr unsigned US = UFL ? 4096u : 2048u;
#define ULD() do { ABL("dwordx4", u[0], ub, 0); ABLS("dwordx4", u[1], ub, US); if (TH) ABLS("dwordx4", u[2], ub, 2 * US); } while (0)
            if (DU) wait_until(rt32() + DU);
            ULD();
            lds_signal<M_CNT0 + 4>();
            lds_wait<M_CNT0 + 4>(4u);
            ST8(((u32x2){tag, rt32()}), rs, c2o);
            CFENCE();
            if (owner) MPLD();
            ST16(SENT4, rs, yo); ST16(SENT4, rs, yo + 4096);
            CFENCE();
            if (is_row) XLD();
            if (is_row || owner) VMW(7); else VMW(3);
            if (TH) {
                unsigned bad;
#define UIN() do { PIN(u[0]); PIN(u[1]); PIN(u[2]); bad = 0; \
                    _Pragma("unroll") for (int i = 0; i < 3; ++i) { \
                        LDS(unsigned, ul3[i]) = u[i][0]; LDS(unsigned, ul3[i] + 4) = u[i][2]; \
                        LDS(unsigned, ux3[i]) = __builtin_amdgcn_perm(u[i][3], u[i][1], 0x05040100u); \
                        bad |= __builtin_amdgcn_perm(u[i][3], u[i][1], 0x07060302u) ^ tag; } } while (0)
                UIN();
                if (__builtin_expect(!none(bad != 0u), 0)) {
                    SPIN {
                        STAMP(23);
                        ULD(); VMW(0); UIN();
                        if (none(bad != 0u)) break;
                    }
                }
            } else {
                PIN(u[0]); PIN(u[1]);
                LDS(u32x4, ul) = u[0]; LDS(u32x4, ul + 512) = u[1];
            }
        }
        BAR();
        STAMP(5);
        const unsigned tconv = rt32();
        if (owner) {
            VMW(2);
            PIN(xr[0]); PIN(xr[1]); PIN(xr[2]); PIN(xr[3]);
            SPIN {
                if (none(max4(xr[0], max4(xr[1], max4(xr[2], max4(xr[3], 0u)))) == SENT)) break;
                MPLD();
                VMW(0); PIN(xr[0]); PIN(xr[1]); PIN(xr[2]); PIN(xr[3]);
            }
            float s4[4];
#pragma unroll
            for (int e = 0; e < 4; ++e)
                s4[e] = sadd(sadd(__uint_as_float(xr[0][e]), __uint_as_float(xr[1][e])), sadd(__uint_as_float(xr[2][e]), __uint_as_float(xr[3][e])));
            asm volatile("s_nop 1" : "+v"(s4[0]), "+v"(s4[1]), "+v"(s4[2]), "+v"(s4[3]));
            float s0 = fold2(s4[0], s4[2]); PIN(s0);
            float s1 = fold2(s4[1], s4[3]); PIN(s1);
            s0 = dpp_add<0x118>(s0); s1 = dpp_add<0x118>(s1);
            s0 = sadd(s0, __shfl_xor(s0, 16)); s1 = sadd(s1, __shfl_xor(s1, 16));
            ST8(((u32x2){pk2(s0, s1), tag}), rs, (lane & 24) == 8 ? WS_MOD + (192 * G + 16 * f + 2 * (lane & 7) + (lane >> 5)) * 8 : WS_END);
            VMW(1);
        } else if (is_row) VMW(4); else VMW(0);
        if (lds_arrive<M_CNT0 + 8>() == 3u) ST4(tag, rs, lane == 0 ? WS_F4 + (G * 32 + j) * F4S : WS_END);
        u32x2 fl;
        unsigned pf = 0, pd = 0;
        if (is_row) ABL("dwordx2", fl, f2o, 0);
        else { AGL("dword", pf, pfa, pfo, 0); if (PDW) AGL("dword", pd, wd, pdo, 0); }
        STAMP(18);
        PIN(dw); PIN(ww);
        const float Dv = blo(dw);
        float kd[2][8];
#pragma unroll
        for (int i = 0; i < 2; ++i) {
            const u32x4 kv = LDS(u32x4, kvo + i * 2048);
#pragma unroll
            for (int e = 0; e < 4; ++e) { kd[i][2 * e] = smul(bhi(kv[3 - e]), Dv); kd[i][2 * e + 1] = smul(blo(kv[3 - e]), Dv); PIN(kd[i][2 * e]); PIN(kd[i][2 * e + 1]); }
        }
        CFENCE();
        u32x2 m[6];
        unsigned tl = 0;
        bool early = false;
#define MODLD() do { ABL("dwordx2", m[0], mo, 0); ABL("dwordx2", m[1], mo, 2048); ABLS("dwordx2", m[2], mo, 4096u); ABLS("dwordx2", m[3], mo, 6144u); \
                     ABLS("dwordx2", m[4], mo, 8192u); ABLS("dwordx2", m[5], mo, 10240u); } while (0)
#define MODPIN() do { PIN(m[0]); PIN(m[1]); PIN(m[2]); PIN(m[3]); PIN(m[4]); PIN(m[5]); } while (0)
#define F2OK() ((__builtin_amdgcn_ballot_w64(fl[0] != tag) & 0xffull) == 0ull)
#define F2MAX() (row_max(lane < 8 ? fl[1] - tconv + 0x40000000u : 0u) + tconv - 0x40000000u)
#define TLOUT() do { LDS(u32x2, M_TY) = (u32x2){tl, (int)(tl + DF - tconv) < DE ? 2u : 1u}; } while (0)
        if (is_row) {
            VMW(0); PIN(fl);
            if (!F2OK()) ABL("dwordx2", fl, f2o, 0);
            { const u32x4 x0 = LDS(u32x4, xo_), x1 = LDS(u32x4, xo_ + 1024); XUNPACK(x0, x1); }
            STAMP(19);
            VMW(0); PIN(fl);
#pragma unroll 1
            while (!F2OK() && (int)(rt32() - tconv) < DW - 30) { ABL("dwordx2", fl, f2o, 0); VMW(0); PIN(fl); }
            if (F2OK()) {
                tl = F2MAX();
                TLOUT();
                if ((int)(tl + DM - tconv) < DW) { wait_until(tl + DM); MODLD(); early = true; }
            }
        }
        {
            STAMP(6);
            BAR();
            STAMP(7);
            f32x4 p[2][4];
#pragma unroll
            for (int i = 0; i < 2; ++i) {
                p[i][0] = LDS(f32x4, pr + i * 4608); p[i][1] = LDS(f32x4, pr + i * 4608 + 9216);
                p[i][2] = LDS(f32x4, pr + i * 4608 + 288); p[i][3] = LDS(f32x4, pr + i * 4608 + 9216 + 288);
            }
#pragma unroll
            for (int i = 0; i < 2; ++i) {
                u32x4 r;
#pragma unroll
                for (int e = 0; e < 4; ++e) {
                    const f32x4 pa = p[i][e & 2], pb = p[i][(e & 2) + 1];
                    const f32x2 v = ((f32x2){pa[2 * (e & 1)], pa[2 * (e & 1) + 1]} + (f32x2){kd[i][2 * e], kd[i][2 * e + 1]}) + (f32x2){pb[2 * (e & 1)], pb[2 * (e & 1) + 1]};
                    r[e] = pk2(v[0], v[1]);
                }
                ST16(r, rs, yo + i * 4096);
            }
        }
        CFENCE();
        STAMP(8);
        VMW(2);
        KEEP(pf); KEEP(pd);
        if (is_row) {
            if (!early) {
                SPIN {
                    if (F2OK()) break;
                    ABL("dwordx2", fl, f2o, 0);
                    VMW(0); PIN(fl);
                }
                tl = F2MAX();
                TLOUT();
                wait_until(tl + DM);
                STAMP(22);
                MODLD();
                VMW(0);
            }
            MODPIN();
            SPIN {
                unsigned bad = 0;
#pragma unroll
                for (int i = 0; i < 6; ++i) bad |= m[i][1] ^ tag;
                if (__builtin_expect(none(bad != 0u), 1)) break;
                STAMP(21);
                __builtin_amdgcn_s_sleep(4);
                MODLD();
                VMW(0); MODPIN();
            }
            STAMP(17);
            const f32x2 one = {1.f, 1.f};
            const f32x2 ca = b2(m[1][0]) + one, cb = b2(m[4][0]) + one, ga = b2(m[2][0]), sb = b2(m[3][0]), w = b2(ww);
            const f32x2 ca1 = b2(pk2(ca[0], ca[1])), cb1 = b2(pk2(cb[0], cb[1]));
            const f32x2 wc = w * cb1, sw = sb * w;
            LDS(f32x2, L_CH + t * 8) = ga * ca1;
            LDS(f32x2, L_CH + 2048 + t * 8) = ga * b2(m[0][0]);
            LDS(f32x2, L_CH + 4096 + t * 8) = wc;
            LDS(f32x2, L_CH + 6144 + t * 8) = cb1;
            LDS(f32x2, L_CH + 8192 + t * 8) = sb;
            LDS(f32x2, L_CH + 10240 + t * 8) = b2(m[5][0]);
            const float s = half_sum(fold2(wc[0] + wc[1], sw[0] + sw[1]));
            if ((lane & 31) == 31) LDS(float, M_MODP + (lane >> 5) * 16 + hw * 4) = s;
        }
    } else {
        const int cw = wave - 4, tc = t - 256;
        const int chl = cw >> 1, h = cw & 1;
        u32x4 A[4][8];
        {
            const unsigned t0 = rt32();
            unsigned tch;
            if (TOUCH) {
                const void* tp = cw == 0 ? (const void*)ws : cw == 1 ? (const void*)out : is_row ? (const void*)xq : f < 7 ? (const void*)(cw == 2 ? wg : wu) : (const void*)wd;
                const unsigned to = cw == 0 ? (lane == 0 ? WS_Y - 128 : lane == 1 ? WS_H - 128 : WS_END - 128)
                                  : is_row ? rbk * 16384u : (lane & 1) * (unsigned)(D * F * 2 - 128);
                AGL("dword", tch, tp, to, 0);
            }
            const unsigned mpo = a < 6 ? WS_MP + (G * 32 + j) * 1536 + cw * 384 + a * 64 + g * 16 : WS_END;
            ST16(SENT4, rs, mpo);
            const unsigned so = is_row ? (tc < 4 ? WS_R + rbk * 128 + tc * 16 : WS_END) : ((f < 7 && tc < 32) ? WS_ACT + (G * 7 + f) * 512 + tc * 16 : WS_END);
            ST16(SENT4, rs, (PT && (tc >> 3) == 16) ? WS_F1 + (G * 32 + j) * 128 + (tc & 7) * 16 : so);
            CFENCE();
            if (cw == 0) LDS(u32x4, L_U + (lane >> 5) * U_STRIDE + (lane & 31) * 16) = (u32x4){0, 0, 0, 0};
            if (cw == 1) LDS(u32x4, M_TY) = (u32x4){0, 0, 0, 0};
            const auto rk = __builtin_amdgcn_make_buffer_rsrc((void*)(ssmk + (ch0 + chl) * S), 0, S * 2, 0x00020000);
            unsigned ko = g * 512 + h * 256 + a * 16, ko2 = (lane >> 2) * 512 + h * 256 + (lane & 3) * 16 - 32;
            unsigned wo = (16 * j + (lane >> 2)) * 6144 + 768 * G + 192 * cw + (lane & 3) * 16;
            PIN(ko); PIN(ko2); PIN(wo);
            if (DK) wait_until(t0 + DK);
            CFENCE();
            const u32x4 k0 = __builtin_amdgcn_raw_buffer_load_b128(rk, ko, 0, 0);
            const u32x4 k1 = __builtin_amdgcn_raw_buffer_load_b128(rk, ko + 2048, 0, 0);
            const u32x4 k2 = __builtin_amdgcn_raw_buffer_load_b128(rk, ko2, 0, 0);
            const u32x2 cv = GLD(u32x2, cin, 32 * j + 8 * g);
            const u32x4 m0 = GLD(u32x4, wmod, wo), m1 = GLD(u32x4, wmod, wo + 64), m2 = GLD(u32x4, wmod, wo + 128);
            CFENCE();
            VMW(7);
            tag = mktag(rfl(ep));
            if (lds_arrive<M_CNT0>() == 7u) ST4(tag, rs, lane == 0 ? WS_F1 + (G * 32 + j) * 128 : WS_END);
            KEEP(cpf);
            if (TOUCH) KEEP(tch);
            STAMP(1);
            const unsigned kc = L_KC + cw * KCW;
            const unsigned w0 = kc + g * 288 + 32 + a * 16, w2 = kc + (lane >> 2) * 288 + (lane & 3) * 16;
#define SHF(k, n) ((u32x4){__builtin_amdgcn_alignbit(k[1], k[0], 16), __builtin_amdgcn_alignbit(k[2], k[1], 16), __builtin_amdgcn_alignbit(k[3], k[2], 16), __builtin_amdgcn_alignbit(n, k[3], 16)})
#define NXT(k) ((unsigned)__builtin_amdgcn_update_dpp(0, (int)k[0], 0x101, 0xf, 0xf, true))
            LDS(u32x4, w0) = k0;
            LDS(u32x4, w0 + KC1) = SHF(k0, NXT(k0));
            LDS(u32x4, w0 + 1152) = k1; LDS(u32x4, w0 + 1152 + KC1) = SHF(k1, NXT(k1));
            const u32x4 k2s = SHF(k2, NXT(k2));
            if (lane < 32 && (lane & 2) == 0) { LDS(u32x4, w2) = k2; LDS(u32x4, w2 + KC1) = k2s; }
            const u32x2 sc = {pk2(silu(blo(cv[0])), silu(bhi(cv[0]))), pk2(silu(blo(cv[1])), silu(bhi(cv[1])))};
            const unsigned ml = L_T + cw * 3072 + (lane >> 2) * 192;
            LDS(u32x4, ml + (lane & 3) * 16) = m0;
            LDS(u32x4, ml + (lane & 3) * 16 + 64) = m1;
            LDS(u32x4, ml + (lane & 3) * 16 + 128) = m2;
            STAMP(2);
            f32x4 ms = {0, 0, 0, 0};
#pragma unroll
            for (int i = 0; i < 6; ++i)
                ms = mfma16(trd(ml + (lane & 3) * 8 + i * 32), a == i ? sc : (u32x2){0u, 0u}, ms);
            ST16(__builtin_bit_cast(u32x4, ms), rs, mpo);
            CFENCE();
            STAMP(3);
            const int x0_ = 25 + a - 8 * g;
            const int cpy = x0_ & 1;
            const unsigned base = kc + cpy * KC1 + (x0_ - cpy) * 2;
#pragma unroll
            for (int mu = 0; mu < 8; ++mu)
#pragma unroll
                for (int r = 0; r < 4; ++r) {
                    const LAS unsigned* p = (const LAS unsigned*)(base + mu * 288 + r * 64);
                    A[r][mu] = (u32x4){p[0], p[1], p[2], p[3]};
                }
#pragma unroll
            for (int r = 0; r < 4; ++r)
#pragma unroll
                for (int mu = 0; mu < 8; ++mu) PIN(A[r][mu]);
        }
        unsigned base = L_U + chl * U_STRIDE + U_PADB + (16 * (a - 1) + 8 * g - h * 8 * 16 - 96) * 2;
        unsigned pw = L_P + cw * 9216 + chl * 128 + g * 288 + a * 16;
        unsigned f4o = WS_F4 + tc * F4S, lno = M_LN1 + (2 * cw + (g & 1)) * 16 + (g >> 1) * 4;
        yl = WS_Y + (cw * 64 + (lane >> 2)) * 8192 + rbk * 64 + (lane & 3) * 16;
#pragma unroll
        for (int d = 0; d < 4; ++d) { tpo[d] = L_T + ((lane & 1) * 4 + d) * 2064 + (cw * 128 + (lane >> 1)) * 4; PIN(tpo[d]); }
        ROWADDR();
        PIN(base); PIN(pw); PIN(f4o); PIN(lno); PIN(yl);
        f32x4 acc[8];
#pragma unroll
        for (int i = 0; i < 8; ++i) acc[i] = (f32x4){0, 0, 0, 0};
        STAMP(4);
        __builtin_amdgcn_s_setprio(3);
        BAR();
        STAMP(5);
        u32x4 B[2][8];
#pragma unroll
        for (int tau = 0; tau < 8; ++tau) B[0][tau] = LDS(u32x4, base + (256 * tau + 96) * 2);
        SCHED();
#pragma unroll
        for (int r = 0; r < 3; ++r) {
#pragma unroll
            for (int tau = 0; tau < 8; ++tau) {
#pragma unroll
                for (int mu = 0; mu + tau < 8; ++mu) {
                    acc[mu + tau] = mfma(A[r][mu], B[r & 1][tau], acc[mu + tau]);
                    if (tau == 1 || (tau == 2 && mu == 0)) {
                        const int n = tau == 1 ? mu : 7;
                        B[(r + 1) & 1][n] = LDS(u32x4, base + (256 * n + 96 - 32 * (r + 1)) * 2);
                        SCHED();
                    }
                }
                SCHED();
            }
        }
        u32x2 tw;
        unsigned f4;
#pragma unroll
        for (int tt = 0; tt < 8; ++tt) {
#pragma unroll
            for (int mu = 0; mu <= tt; ++mu) {
                acc[tt] = mfma(A[3][mu], B[1][tt - mu], acc[tt]);
                if (mu == tt && tt >= 2) LDS(f32x4, pw + (tt - 2) * 1152) = acc[tt - 2];
                if (tt == 7 && mu == 3) LDS(f32x4, pw + 6 * 1152) = acc[6];
                if (tt == 6 && mu == 2) tw = LDS(volatile u32x2, M_TY);
                if (tt == 7 && mu == 2) ABL("dword", f4, tw[1] == 2u ? f4o : WS_END, 0);
                SCHED();
            }
        }
        LDS(f32x4, pw + 7 * 1152) = acc[7];
        __builtin_amdgcn_s_setprio(0);
        STAMP(6);
        BAR();
        STAMP(7);
        if (is_row) {
            u32x4 xk0, xk1;
            {
                u32x4 xv[4];
                float s[4] = {0, 0, 0, 0}, q[4] = {0, 0, 0, 0};
#pragma unroll
                for (int i = 0; i < 4; ++i) xv[i] = LDS(u32x4, xo_ + i * 1024);
#pragma unroll
                for (int e = 0; e < 4; ++e)
#pragma unroll
                    for (int i = 0; i < 4; ++i) { s[i] = dot2(xv[i][e], 0x3f803f80u, s[i]); q[i] = dot2(xv[i][e], xv[i][e], q[i]); }
                const float mv = row_sum(fold16(fold2(s[0], s[1]), fold2(s[2], s[3]))) * (1.f / 512.f);
                const float qv = row_sum(fold16(fold2(q[0], q[1]), fold2(q[2], q[3]))) * (1.f / 512.f);
                const float var = rb(fmaxf(qv - mv * mv, 0.f));
                const float rstd = __builtin_amdgcn_rcpf(rb(__builtin_amdgcn_sqrtf(rb(var + 1e-5f))));
                if (a == 15) { LDS(float, lno) = -rb(mv) * rstd; LDS(float, lno + 8) = rstd; }
                xk0 = xv[2]; xk1 = xv[3];
            }
            STAMP(22);
            VMW(0); PIN(f4);
            if (!none(f4 != tag)) {
                if (__builtin_amdgcn_readfirstlane(tw[1]) == 0u) { lds_wait<M_TY + 4>(1u); tw = LDS(volatile u32x2, M_TY); }
                wait_until(__builtin_amdgcn_readfirstlane(tw[0]) + DF);
                SPIN {
                    ABL("dword", f4, f4o, 0);
                    VMW(0); PIN(f4);
                    if (none(f4 != tag)) break;
                    STAMP(17);
                    __builtin_amdgcn_s_sleep(2);
                }
            }
            STAMP(8);
            wait_until(__builtin_amdgcn_readfirstlane(tw[0]) + D2);
            YLD();
            STAMP(23);
            XUNPACK(xk0, xk1);
            __builtin_amdgcn_s_setprio(1);
        } else { VMW(0); KEEP(f4); }
    }
    if (is_row) {
        f32x2 gh[8];
        STAMP(9);
        BAR();
        STAMP(10);
        f32x4 wc[2];
        {
            const f32x4 st = LDS(f32x4, M_LN1 + rp * 16);
            f32x4 A1[2], A0[2];
#pragma unroll
            for (int hh = 0; hh < 2; ++hh) { A1[hh] = LDS(f32x4, cho + hh * 16); A0[hh] = LDS(f32x4, cho + 2048 + hh * 16); }
#pragma unroll
            for (int hh = 0; hh < 2; ++hh) wc[hh] = LDS(f32x4, cho + 4096 + hh * 16);
            SCHED();
            const f32x2 nm = {st[0], st[1]}, rs1 = {st[2], st[3]};
#pragma unroll
            for (int e = 0; e < 8; ++e) {
                const float a1 = A1[e >> 2][e & 3], a0 = A0[e >> 2][e & 3];
                gh[e] = pfma(pfma(x[e], rs1, nm), (f32x2){a1, a1}, (f32x2){a0, a0});
            }
#pragma unroll
            for (int e = 0; e < 8; ++e) PIN(gh[e]);
            PIN(wc[0]); PIN(wc[1]);
        }
        float Wc = 0, Sb = 0;
        if (wave == 0) {
            const f32x4 w4 = LDS(f32x4, M_MODP), s4 = LDS(f32x4, M_MODP + 16);
            Wc = (w4[0] + w4[1]) + (w4[2] + w4[3]); Sb = (s4[0] + s4[1]) + (s4[2] + s4[3]);
        }
        PIN(Wc); PIN(Sb);
        if (wave >= 4) {
            STAMP(19);
            SPIN {
                VMW(2); PIN(y[0]); PIN(y[1]);
#pragma unroll
                for (int d = 0; d < 4; ++d) { LDS(unsigned, tpo[d]) = y[0][d]; LDS(unsigned, tpo[d] + 128) = y[1][d]; }
                const unsigned mx = max4(y[0], max4(y[1], 0u));
                VMW(0); PIN(y[2]); PIN(y[3]);
#pragma unroll
                for (int d = 0; d < 4; ++d) { LDS(unsigned, tpo[d] + 256) = y[2][d]; LDS(unsigned, tpo[d] + 384) = y[3][d]; }
                if (__builtin_expect(none(max4(y[2], max4(y[3], mx)) == SENT), 1)) break;
                STAMP(21);
                __builtin_amdgcn_s_sleep(2);
                YLD();
            }
            VMW(0);
            __builtin_amdgcn_s_setprio(0);
        }
        STAMP(11);
        BAR();
        STAMP(20);
        float mv, rstd;
        {
            const u32x4 ya = LDS(u32x4, tro), yc = LDS(u32x4, tro + 16);
            f32x2 s1, s2, s3;
#pragma unroll
            for (int e = 0; e < 8; ++e) {
                x[e] = pfma(gh[e], b2(e < 4 ? ya[e & 3] : yc[e & 3]), x[e]);
                const float w = wc[e >> 2][e & 3];
                if (e == 0) { s1 = x[0]; s2 = x[0] * x[0]; s3 = x[0] * w; }
                else { s1 += x[e]; s2 = pfma(x[e], x[e], s2); s3 = pfma(x[e], (f32x2){w, w}, s3); }
            }
            const float v1 = half_sum(fold2(s1[0], s1[1])), v2 = half_sum(fold2(s2[0], s2[1])), v3 = half_sum(fold2(s3[0], s3[1]));
            if ((lane & 31) == 31) { LDS(float, ko) = v1; LDS(float, ko + 64) = v2; LDS(float, ko + 128) = v3; }
        }
        BAR();
        {
            mv = LDS(float, kr) * (1.f / 512.f);
            const float q = LDS(float, kr + 64) * (1.f / 512.f);
            const float s = LDS(float, kr + 128);
            const float var = rb(fmaxf(q - mv * mv, 0.f));
            rstd = __builtin_amdgcn_rcpf(rb(__builtin_amdgcn_sqrtf(rb(var + 1e-5f))));
            if (wave == 0) {
                const float r = rstd * (s - mv * Wc) + Sb;
                const int u = (int)(pk2(r, r) & 0xffff0000u);
                const unsigned ord = ((unsigned)u ^ ((unsigned)(u >> 31) | 0x80000000u)) >> 16;
                ST4((ord << 12) + kb_, rs, kg);
            }
        }
        CFENCE();
        STAMP(12);
        const auto ro = __builtin_amdgcn_make_buffer_rsrc(out, 0, S * D * 2, 0x00020000);
        {
            const float nmv = -mv * rstd;
            const f32x2 r2 = {rdl(rstd, 2 * rp), rdl(rstd, 2 * rp + 1)}, n2 = {rdl(nmv, 2 * rp), rdl(nmv, 2 * rp + 1)};
            f32x2 h2[8];
            f32x4 gb[2];
#pragma unroll
            for (int hh = 0; hh < 2; ++hh) {
                const f32x4 cb1 = LDS(f32x4, cho + 6144 + hh * 16), sb = LDS(f32x4, cho + 8192 + hh * 16);
                gb[hh] = LDS(f32x4, cho + 10240 + hh * 16);
#pragma unroll
                for (int e4 = 0; e4 < 4; ++e4)
                    h2[hh * 4 + e4] = pfma(pfma(x[hh * 4 + e4], r2, n2), (f32x2){cb1[e4], cb1[e4]}, (f32x2){sb[e4], sb[e4]});
            }
            const unsigned oo = (rbk * 16 + 2 * rp) * 1024 + lane * 16;
            const unsigned ho = WS_H + (rbk * 16 + 2 * rp) * 1536;
#pragma unroll
            for (int i = 0; i < 2; ++i) {
                const unsigned d2 = pk2(h2[4][i], h2[5][i]);
                ST16(((u32x4){pk2(h2[0][i], h2[1][i]), __builtin_amdgcn_perm(tag, d2, 0x05040100u), pk2(h2[2][i], h2[3][i]), __builtin_amdgcn_perm(tag, d2, 0x07060302u)}), rs, ho + i * 1536 + lane * 16);
                ST8(((u32x2){pk2(h2[6][i], h2[7][i]), tag}), rs, ho + i * 1536 + 1024 + lane * 8);
            }
#pragma unroll
            for (int e = 0; e < 8; ++e) { const float gv = gb[e >> 2][e & 3]; x[e] = pfma(h2[e], (f32x2){gv, gv}, x[e]); }
            if (ODEL) __builtin_amdgcn_s_sleep(ODEL);
#pragma unroll
            for (int i = 0; i < 2; ++i)
                ST16(((u32x4){pk2(x[0][i], x[1][i]), pk2(x[2][i], x[3][i]), pk2(x[4][i], x[5][i]), pk2(x[6][i], x[7][i])}), ro, oo + i * 1024);
        }
        DRAIN();
        BAR();
        if (t == 0) ST4(tag, rs, WS_F3 + rbk * 128);
        STAMP(13);
    } else {
        if (f >= 7) {
            if (!PDW) { const unsigned pd = GLD(unsigned, wd, min(200 * (f - 7) + min(t, 199), F - 1) * (D * 2) + 128 * G); KEEP(pd); }
            if (f >= 11) { STAMP_OUT(); return; }
        }
        const bool is_gu = f < 7;
        u32x4 Wf[8];
        {
            const unsigned wl = L_W + wave * 8192;
            if (is_gu) {
                const u16* wp = wave < 4 ? wg : wu;
                const unsigned wo = ((wave & 3) * 128 + (lane >> 2)) * (F * 2) + (224 * G + 32 * f + 8 * (lane & 3)) * 2;
                u32x4 W[8];
#pragma unroll
                for (int i = 0; i < 8; ++i) W[i] = GLD(u32x4, wp, wo + i * 16 * (F * 2));
#pragma unroll
                for (int i = 0; i < 8; ++i) LDS(u32x4, wl + i * 1024 + lane * 16) = W[i];
                const unsigned rp = wl + (8 * g + (a >> 2)) * 64 + (lane & 3) * 8;
#pragma unroll
                for (int c = 0; c < 2; ++c)
#pragma unroll
                    for (int ks = 0; ks < 4; ++ks) Wf[c * 4 + ks] = trd2(rp + ks * 2048 + c * 32, 256);
            } else {
                const unsigned wo = (wave * 224 + (lane >> 1)) * (D * 2) + (64 * G + 16 * (f - 7) + 8 * (lane & 1)) * 2;
                u32x4 W[7];
#pragma unroll
                for (int i = 0; i < 7; ++i) W[i] = GLD(u32x4, wd, wo + i * 32 * (D * 2));
#pragma unroll
                for (int i = 0; i < 7; ++i) LDS(u32x4, wl + i * 1024 + lane * 16) = W[i];
                const unsigned rp = wl + (8 * g + (a >> 2)) * 32 + (lane & 3) * 8;
#pragma unroll
                for (int ks = 0; ks < 7; ++ks) Wf[ks] = trd2(rp + ks * 1024, 128);
                Wf[7] = Wf[0];
            }
#pragma unroll
            for (int i = 0; i < 8; ++i) PIN(Wf[i]);
        }
        STAMP(9);
        unsigned ro_ = WS_R + (t >> 2) * 128 + (t & 3) * 16, cko = M_CAND + (t >> 3) * 4, cco = M_CAND + (t & 7) * 32;
        unsigned cwo = M_CAND + (t >> 4) * 8;
        unsigned xw = L_X + wave * 1040 + lane * 16, hb = WS_H + lane * 16, hb2 = WS_H + 1024 + lane * 8, slo = M_SEL2 + wave * 4, xrd = L_X + (a & 7) * 1040 + (wave & 3) * 256 + g * 16;
        unsigned pwo = a >= 8 ? M_DUMP + lane * 16 : is_gu ? L_P + (wave * 8 + a) * 144 + g * 16 : L_P + (wave * 8 + a) * 80 + g * 16;
        unsigned pp = is_gu ? L_P + (lane >> 3) * 144 + ((wave & 3) * 8 + (lane & 7)) * 4 : L_P + (lane >> 3) * 80 + (lane & 7) * 8;
        unsigned ao = is_gu ? ((t & 1) ? WS_END : WS_ACT + (G * 7 + f) * 512 + t * 2) : (a < 8 ? WS_ACT + wave * 7 * 512 + g * 128 + a * 16 : WS_END);
        PIN(ro_); PIN(cko); PIN(cco); PIN(cwo); PIN(xw); PIN(xrd); PIN(hb); PIN(hb2); PIN(slo); PIN(pwo); PIN(pp); PIN(ao);
        unsigned tlf;
        {
            u32x2 fl;
            SPIN {
                CFENCE();
                fl = LD8(rs, lane < 8 ? WS_F2 + (G * 8 + lane) * 128 : WS_END);
                if ((__builtin_amdgcn_ballot_w64(fl[0] != tag) & 0xffull) == 0ull) break;
                __builtin_amdgcn_s_sleep(4);
            }
            const unsigned now = rt32();
            tlf = row_max(lane < 8 ? fl[1] - now + 0x40000000u : 0u) - 0x40000000u + now;
            if (D3) wait_until(tlf + D3);
        }
        STAMP(10);
        u32x4 key;
        unsigned k0;
        {
            unsigned sp = SPIN_CAP;
#pragma unroll 1
            do {
                ABL("dwordx4", key, ro_, 0);
                VMW(0); PIN(key);
                k0 = max4(key, 0u);
            } while (!none(k0 == SENT) && --sp != 0u);
        }
        STAMP(11);
#define RANK() do { \
            const unsigned kx = LDS(unsigned, cko); \
            const u32x4 c0 = LDS(u32x4, cco), c1 = LDS(u32x4, cco + 16); \
            unsigned bits = 0; \
            _Pragma("unroll") for (int e = 0; e < 4; ++e) bits = __builtin_amdgcn_alignbit(bits, kx - c0[e], 31); \
            _Pragma("unroll") for (int e = 0; e < 4; ++e) bits = __builtin_amdgcn_alignbit(bits, kx - c1[e], 31); \
            int rank = __builtin_popcount(bits); \
            rank += __builtin_amdgcn_update_dpp(0, rank, 0xb1, 0xf, 0xf, true);      \
            rank += __builtin_amdgcn_update_dpp(0, rank, 0x4e, 0xf, 0xf, true);      \
            rank += __builtin_amdgcn_update_dpp(0, rank, 0x141, 0xf, 0xf, true);     \
            LDS(unsigned, M_SEL2 + rank * 4) = kx;        } while (0)
        unsigned k1, k2, k3;
        {
            const unsigned a0 = max(key[0], key[1]), a1 = min(key[0], key[1]), a2 = max(key[2], key[3]), a3 = min(key[2], key[3]);
            const unsigned m1 = min(a0, a2), m2 = max(a1, a3);
            k1 = max(m1, m2); k2 = min(m1, m2); k3 = min(a1, a3);
        }
        const unsigned r1 = row_allmax(k0);
        const bool p1 = k0 == r1;
        const unsigned j0 = p1 ? k1 : k0, j1 = p1 ? k2 : k1;
        const unsigned r2 = row_allmax(j0);
        LDS(u32x2, cwo) = (u32x2){r1, r2};
        unsigned rem = j0 == r2 ? j1 : j0;
        BAR();
        RANK();
        BAR();
        STAMP(12);
#define REDO() do { \
            const unsigned T_ = __builtin_amdgcn_readfirstlane(LDS(unsigned, M_SEL2 + 28)); \
            BAR(); \
            if (lane < 8) LDS(unsigned, M_CAND + (wave * 8 + lane) * 4) = wave * 8 + lane; \
            _Pragma("unroll 1") for (int i = 0; i < 8; ++i) { \
                const unsigned mx = wave_max(k0); \
                if (mx < T_) break; \
                if (k0 == mx) { LDS(unsigned, M_CAND + (wave * 8 + i) * 4) = mx; k0 = k1; k1 = k2; k2 = k3; k3 = 0u; } \
            } \
            BAR(); RANK(); BAR(); } while (0)
        if (is_gu) {
            float v[8];
            unsigned redo;
#define GU() do { \
                const unsigned sr = LDS(unsigned, slo) & 0x7ffu, t8 = LDS(unsigned, M_SEL2 + 28); \
                u32x4 hv; u32x2 hw_; \
                ABL("dwordx4", hv, sr * 1536u + hb, 0); ABL("dwordx2", hw_, sr * 1536u + hb2, 0); \
                unsigned fb = rem > t8 ? 1u : 0u; \
                PIN(fb); \
                VMW(0); PIN(hv); PIN(hw_); \
                LDS(u32x4, xw) = (u32x4){hv[0], hv[2], __builtin_amdgcn_perm(hv[3], hv[1], 0x05040100u), hw_[0]}; \
                if (((__builtin_amdgcn_perm(hv[3], hv[1], 0x07060302u) ^ tag) | (hw_[1] ^ tag)) != 0u) fb |= 2u; \
                STAMP(13); \
                BAR(); \
                u32x4 xa[4]; \
                _Pragma("unroll") for (int ks = 0; ks < 4; ++ks) xa[ks] = LDS(u32x4, xrd + ks * 64); \
                SCHED(); \
                if (fb != 0u) __hip_atomic_fetch_or((LAS unsigned*)M_REDO, fb, __ATOMIC_RELAXED, __HIP_MEMORY_SCOPE_WORKGROUP); \
                f32x4 acc0 = {0, 0, 0, 0}, acc1 = {0, 0, 0, 0}; \
                _Pragma("unroll") for (int ks = 0; ks < 4; ++ks) { acc0 = mfma(Wf[ks], xa[ks], acc0); acc1 = mfma(Wf[4 + ks], xa[ks], acc1); } \
                  \
                LDS(f32x4, pwo) = acc0; LDS(f32x4, pwo + 64) = acc1; \
                BAR(); \
                _Pragma("unroll") for (int w = 0; w < 8; ++w) v[w] = LDS(float, pp + w * 1152); \
                redo = LDS(unsigned, M_REDO); \
                SCHED(); } while (0)
#define ACT() do { \
                const float gp = (v[0] + v[1]) + (v[2] + v[3]), up = (v[4] + v[5]) + (v[6] + v[7]); \
                const float o = silu(gp) * up; \
                const float on = __int_as_float(__builtin_amdgcn_update_dpp(0, __float_as_int(o), 0xb1, 0xf, 0xf, true)); \
                ST4(pk2(o, on), rs, redo == 0u ? ao : WS_END); } while (0)
            GU();
            if (wave < 4) ACT();
            STAMP(14);
            if (__builtin_expect(__builtin_amdgcn_readfirstlane(redo) != 0u, 0)) {
                SPIN {
                    if (__builtin_amdgcn_readfirstlane(redo) & 1u) { REDO(); rem = 0u; } else BAR();
                    if (t == 0) LDS(unsigned, M_REDO) = 0u;
                    GU();
                    if (__builtin_amdgcn_readfirstlane(redo) == 0u) break;
                }
                redo = 0u;
                if (wave < 4) ACT();
            }
        } else {
            const unsigned tk = rt32();
            if (rem > LDS(unsigned, M_SEL2 + 28)) LDS(unsigned, M_REDO) = 1u;
            BAR();
            if (__builtin_expect(__builtin_amdgcn_readfirstlane(LDS(unsigned, M_REDO)) != 0u, 0)) REDO();
            const auto ro = __builtin_amdgcn_make_buffer_rsrc(out, 0, S * D * 2, 0x00020000);
            const int col0 = 64 * G + 16 * (f - 7);
            float g0 = 0, g1 = 0;
            unsigned ooff = 0, oprev;
            if (wave == 0) {
                const unsigned fk = LDS(unsigned, M_SEL2 + (lane >> 3) * 4);
                const int frow = (int)(fk & 0x7ffu);
                unsigned u = (fk >> 12) ^ 0x8000u;
                u = (u & 0x8000u) ? (u ^ 0x7fffu) : u;
                const float sig = rb(__builtin_amdgcn_rcpf(1.f + __expf(-blo(u))));
                u32x2 gbv;
                SPIN {
                    CFENCE();
                    gbv = LD8(rs, WS_MOD + (1280 + col0 / 2 + (lane & 7)) * 8);
                    const unsigned v = LD4(rs, (lane & 7) == 0 ? WS_F3 + (frow >> 4) * 128 : WS_END);
                    if (none((((lane & 7) == 0 ? v ^ tag : 0u) | (gbv[1] ^ tag)) != 0u)) break;
                }
                CFENCE();
                ooff = (frow * D + col0 + 2 * (lane & 7)) * 2;
                asm volatile("global_load_dword %0, %1, %2 sc1" : "=v"(oprev) : "v"(ooff), "s"(out) : "memory");
                g0 = blo(gbv[0]) * sig; g1 = bhi(gbv[0]) * sig;
                PIN(g0); PIN(g1);
            }
            STAMP(21);
            if (DA) wait_until((int)(tk + D4 - tlf - DA) < 0 ? tk + D4 : tlf + DA); else wait_until(tk + D4);
            STAMP(22);
            {
                unsigned sp = SPIN_CAP;
                bool bad;
#pragma unroll 1
                do {
                    u32x4 av[7];
                    ABL("dwordx4", av[0], ao, 0); ABL("dwordx4", av[1], ao, 512); ABL("dwordx4", av[2], ao, 1024); ABL("dwordx4", av[3], ao, 1536);
                    ABL("dwordx4", av[4], ao, 2048); ABL("dwordx4", av[5], ao, 2560); ABL("dwordx4", av[6], ao, 3072);
                    f32x4 acc = {0, 0, 0, 0};
                    unsigned m = 0;
                    VMW(4); PIN(av[0]); PIN(av[1]); PIN(av[2]);
#pragma unroll
                    for (int ks = 0; ks < 3; ++ks) { acc = mfma(Wf[ks], av[ks], acc); m = max4(av[ks], m); }
                    VMW(2); PIN(av[3]); PIN(av[4]);
#pragma unroll
                    for (int ks = 3; ks < 5; ++ks) { acc = mfma(Wf[ks], av[ks], acc); m = max4(av[ks], m); }
                    VMW(0); PIN(av[5]); PIN(av[6]);
#pragma unroll
                    for (int ks = 5; ks < 7; ++ks) { acc = mfma(Wf[ks], av[ks], acc); m = max4(av[ks], m); }
                    bad = !none(m == SENT);
                    LDS(f32x4, pwo) = acc;
                } while (bad && --sp != 0u);
            }
            STAMP(13);
            BAR();
            if (wave == 0) {
                f32x2 v[8];
#pragma unroll
                for (int w = 0; w < 8; ++w) v[w] = LDS(f32x2, pp + w * 640);
                PIN(oprev);
                const f32x2 o = b2(oprev);
                const f32x2 s = ((v[0] + v[1]) + (v[2] + v[3])) + ((v[4] + v[5]) + (v[6] + v[7]));
                ST4(pk2(o[0] + g0 * s[0], o[1] + g1 * s[1]), ro, ooff);
                if (G == 0 && f == 7 && lane == 0) ST4(tag, rs, WS_E);
            }
            STAMP(14);
        }
    }
    STAMP_OUT();
}

extern "C" void kernel_launch(void* const* d_in, const int* in_sizes, int n_in,
                              void* d_out, int out_size, void* d_ws, size_t ws_size,
                              hipStream_t stream) {
    fused_kernel<<<256, 512, 0, stream>>>((const u16*)d_in[1], (const u16*)d_in[2], (char*)d_ws, (const u16*)d_in[4],
                                          (const u16*)d_in[3], (const u16*)d_in[11], (const u16*)d_in[12], (const u16*)d_in[5],
                                          (const u16*)d_in[0], (const u16*)d_in[7], (const u16*)d_in[8], (const u16*)d_in[9],
                                          (const u16*)d_in[10], (u16*)d_out);
}
```
